```python
import torch

def _softmax(x, axis=-1):
    return torch.softmax(x, dim=axis)
def _gelu(x):
    return torch.nn.functional.gelu(x, approximate="tanh")
def _silu(x):
    return torch.nn.functional.silu(x)
def _sigmoid(x):
    return torch.sigmoid(x)
def _layernorm(x, axis=-1, eps=1e-5):
    m = x.mean(dim=axis, keepdim=True)
    v = x.var(dim=axis, keepdim=True, unbiased=False)
    return (x - m) / torch.sqrt(v + eps)
def _scatter_add(tgt, idx, src, axis=0):
    return tgt.clone().index_add_(axis, idx, src)
def _segment_sum(src, seg, n):
    out = torch.zeros((n,) + src.shape[1:], dtype=src.dtype)
    return out.index_add_(0, seg, src)
def _pad(x, pw):
    flat = tuple(v for pair in reversed(pw) for v in pair)
    return torch.nn.functional.pad(x, flat)

kernel_name = 'gated_mix__sliding_block_cached__geglu_ffn__decode'

def setup_inputs():

    _w = (lambda a: (lambda t: t.to(torch.bfloat16) if t.is_floating_point() else t)(torch.as_tensor(a)))
    def _A_setup():
        B, Sq, D = 1, 1, 640
        Hq, Hkv, Sk, Dh = 32, 32, 32768, 192
        assert Hq % Hkv == 0, (Hq, Hkv)
        return {
            "x": _w((lambda *s: torch.randn(*s, dtype=torch.bfloat16) * 0.05)(B, Sq, D)),
            "k_cache": _w((lambda *s: torch.randn(*s, dtype=torch.bfloat16) * 6.4)(B, Hkv, Sk, Dh)),
            "v_cache": _w((lambda *s: torch.randn(*s, dtype=torch.bfloat16) * 0.2)(B, Hkv, Sk, Dh)),
            "wq": _w((lambda *s: torch.randn(*s, dtype=torch.bfloat16) * 6.4)(D, Hq * Dh) * (D ** -0.5)),
            "wo": _w((lambda *s: torch.randn(*s, dtype=torch.bfloat16) * 0.2)(Hq * Dh, D) * ((Hq * Dh) ** -0.5)),
        }
    _A_in = _A_setup()

    def _B_setup():
        B, S, D, F = 1, 1, 640, 1536
        return {
            "x": _w((lambda *s: torch.randn(*s, dtype=torch.bfloat16) * 0.05)(B, S, D)),
            "w_gate": _w((lambda *s: torch.randn(*s, dtype=torch.bfloat16) * 0.05)(D, F)),
            "w_up": _w((lambda *s: torch.randn(*s, dtype=torch.bfloat16) * 0.05)(D, F)),
            "w_down": _w((lambda *s: torch.randn(*s, dtype=torch.bfloat16) * 0.05)(F, D)),
        }
    _B_in = _B_setup()

    return {
        "A_x": _A_in["x"],
        "A_k_cache": _A_in["k_cache"],
        "A_v_cache": _A_in["v_cache"],
        "A_wq": _A_in["wq"],
        "A_wo": _A_in["wo"],
        "B_x": _B_in["x"],
        "B_w_gate": _B_in["w_gate"],
        "B_w_up": _B_in["w_up"],
        "B_w_down": _B_in["w_down"],
        "gate_w": _w((lambda *s: torch.randn(*s, dtype=torch.bfloat16) * 0.1)(640) * 16.0),
    }

def reference(A_x, A_k_cache, A_v_cache, A_wq, A_wo, B_x, B_w_gate, B_w_up, B_w_down, gate_w):
    def _A_ref(x, k_cache, v_cache, wq, wo):
        B, Sq, D = x.shape
        Hq, Dh = 32, 192
        Hkv, Sk = k_cache.shape[1], k_cache.shape[2]
        W = min(4096, max(256, Sk // 4))
        rep = Hq // Hkv
        q = (lambda x, *d: x.permute(*d))((x @ wq).reshape(B, Sq, Hq, Dh), 0, 2, 1, 3)
        k = (lambda x, n, axis: torch.repeat_interleave(x, n, dim=axis))(k_cache[:, :, Sk - W:, :], rep, axis=1)
        v = (lambda x, n, axis: torch.repeat_interleave(x, n, dim=axis))(v_cache[:, :, Sk - W:, :], rep, axis=1)
        scores = torch.einsum("bhqd,bhkd->bhqk", q, k) * (Dh ** -0.5)
        p = _softmax(scores, axis=-1)
        o = torch.einsum("bhqk,bhkd->bhqd", p, v)
        return (lambda x, *d: x.permute(*d))(o, 0, 2, 1, 3).reshape(B, Sq, Hq * Dh) @ wo

    def _B_ref(x, w_gate, w_up, w_down):
        return (_gelu(x @ w_gate) * (x @ w_up)) @ w_down

    x = A_x
    g = _sigmoid((x * gate_w).sum(-1, keepdims=True))
    a = _A_ref(x, A_k_cache, A_v_cache, A_wq, A_wo)
    b = _B_ref(x, B_w_gate, B_w_up, B_w_down)
    return g * a + (1 - g) * b
```

```cpp
#include <hip/hip_runtime.h>
#include <hip/hip_bf16.h>
#include <cstdint>

typedef unsigned int u32;
typedef __attribute__((ext_vector_type(4))) unsigned u32x4;
typedef __attribute__((ext_vector_type(2))) __bf16 bf16x2;

constexpr int D = 640, HQ = 32, DH = 192, SK = 32768, W = 4096, F = 1536;
constexpr int NSPLIT = 8, KEYS = W / NSPLIT;

__device__ __forceinline__ float bf2f(unsigned short v) { return __uint_as_float(((u32)v) << 16); }
__device__ __forceinline__ float lo(u32 v) { return __uint_as_float(v << 16); }
__device__ __forceinline__ float hi(u32 v) { return __uint_as_float(v & 0xffff0000u); }
__device__ __forceinline__ float rbf(float x) { return __bfloat162float(__float2bfloat16(x)); }

__global__ void __launch_bounds__(256) k1(const unsigned short* x, const unsigned short* wq, const unsigned short* wg,
                                          const unsigned short* wu, float* y) {
  __shared__ float red[32][65];
  int b = blockIdx.x;
  const unsigned short* w; int ld, c0;
  if (b < 96) { w = wq; ld = HQ * DH; c0 = b * 64; }
  else if (b < 120) { w = wg; ld = F; c0 = (b - 96) * 64; }
  else { w = wu; ld = F; c0 = (b - 120) * 64; }
  int t = threadIdx.x, r = t >> 3, c = t & 7;
  float acc[8] = {0};
  for (int row = r; row < D; row += 32) {
    u32x4 v = *(const u32x4*)(w + (size_t)row * ld + c0 + c * 8);
    float xv = bf2f(x[row]);
    for (int i = 0; i < 4; i++) { acc[2 * i] += xv * lo(v[i]); acc[2 * i + 1] += xv * hi(v[i]); }
  }
  for (int i = 0; i < 8; i++) red[r][c * 8 + i] = acc[i];
  __syncthreads();
  if (t < 64) { float s = 0; for (int i = 0; i < 32; i++) s += red[i][t]; y[b * 64 + t] = rbf(s); }
}

__global__ void __launch_bounds__(512) k2(const unsigned short* kc, const unsigned short* vc, const float* y, float* part) {
  __shared__ float mrg[8][200];
  int b = blockIdx.x; int h = (b & 7) * 4 + ((b >> 3) & 3), s = b >> 5;
  int t = threadIdx.x, lane = t & 63, wave = t >> 6, r = lane >> 3, c = lane & 7;
  const unsigned short* kb = kc + ((size_t)h * SK + (SK - W) + s * KEYS + wave * 64 + r) * DH + c * 8;
  const unsigned short* vb = vc + ((size_t)h * SK + (SK - W) + s * KEYS + wave * 64 + r) * DH + c * 8;
  float q[24];
  for (int j = 0; j < 3; j++) for (int i = 0; i < 8; i++) q[j * 8 + i] = y[h * DH + j * 64 + c * 8 + i];
  float sc[8];
  float m = -INFINITY;
#pragma unroll
  for (int g = 0; g < 8; g++) {
    float a = 0;
#pragma unroll
    for (int j = 0; j < 3; j++) {
      u32x4 v = __builtin_nontemporal_load((const u32x4*)(kb + g * 8 * DH + j * 64));
      for (int i = 0; i < 4; i++) { a += q[j * 8 + 2 * i] * lo(v[i]); a += q[j * 8 + 2 * i + 1] * hi(v[i]); }
    }
    a += __shfl_xor(a, 1); a += __shfl_xor(a, 2); a += __shfl_xor(a, 4);
    a *= 0.07216878364870322f;
    sc[g] = a; m = fmaxf(m, a);
  }
  m = fmaxf(m, __shfl_xor(m, 8)); m = fmaxf(m, __shfl_xor(m, 16)); m = fmaxf(m, __shfl_xor(m, 32));
  float l = 0, o[24];
  for (int i = 0; i < 24; i++) o[i] = 0;
#pragma unroll
  for (int g = 0; g < 8; g++) {
    float p = __expf(sc[g] - m); l += p;
#pragma unroll
    for (int j = 0; j < 3; j++) {
      u32x4 v = __builtin_nontemporal_load((const u32x4*)(vb + g * 8 * DH + j * 64));
      for (int i = 0; i < 4; i++) { o[j * 8 + 2 * i] += p * lo(v[i]); o[j * 8 + 2 * i + 1] += p * hi(v[i]); }
    }
  }
  l += __shfl_xor(l, 8); l += __shfl_xor(l, 16); l += __shfl_xor(l, 32);
  for (int i = 0; i < 24; i++) { o[i] += __shfl_xor(o[i], 8); o[i] += __shfl_xor(o[i], 16); o[i] += __shfl_xor(o[i], 32); }
  if (r == 0) { for (int j = 0; j < 3; j++) for (int i = 0; i < 8; i++) mrg[wave][j * 64 + c * 8 + i] = o[j * 8 + i];
    if (c == 0) { mrg[wave][192] = m; mrg[wave][193] = l; } }
  __syncthreads();
  if (t < 194) {
    float M = -INFINITY; for (int w = 0; w < 8; w++) M = fmaxf(M, mrg[w][192]);
    float a = 0; for (int w = 0; w < 8; w++) a += __expf(mrg[w][192] - M) * mrg[w][t == 192 ? 193 : t];
    float* p = part + (h * NSPLIT + s) * 200;
    if (t < 192) p[t] = a; else if (t == 192) p[193] = a; else p[192] = M;
  }
}

__global__ void __launch_bounds__(1024) k3(const unsigned short* x, const unsigned short* gw, const unsigned short* wo, const unsigned short* wd,
                                           const float* y, const float* part, __hip_bfloat16* out) {
  __shared__ float z[HQ * DH + F];
  __shared__ float red[128][17];
  __shared__ float gs;
  int t = threadIdx.x;
  for (int i = t; i < HQ * DH; i += 1024) {
    int h = i / DH, d = i % DH; const float* p = part + h * NSPLIT * 200;
    float M = -INFINITY; for (int s = 0; s < NSPLIT; s++) M = fmaxf(M, p[s * 200 + 192]);
    float L = 0, a = 0; for (int s = 0; s < NSPLIT; s++) { float e = __expf(p[s * 200 + 192] - M); L += e * p[s * 200 + 193]; a += e * p[s * 200 + d]; }
    z[i] = rbf(a / L);
  }
  for (int i = t; i < F; i += 1024) {
    float g = y[HQ * DH + i], u = y[HQ * DH + F + i];
    float ge = rbf(0.5f * g * (1.f + tanhf(0.7978845608028654f * (g + 0.044715f * g * g * g))));
    z[HQ * DH + i] = rbf(ge * u);
  }
  if (t < 64) { float s = 0; for (int i = t; i < D; i += 64) s += rbf(bf2f(x[i]) * bf2f(gw[i]));
    for (int k = 1; k < 64; k <<= 1) s += __shfl_xor(s, k);
    if (t == 0) gs = rbf(1.f / (1.f + __expf(-rbf(s)))); }
  __syncthreads();
  int c0 = blockIdx.x * 8;
  float a[8] = {0}, bb[8] = {0};
  for (int row = t; row < HQ * DH; row += 1024) { u32x4 v = *(const u32x4*)(wo + (size_t)row * D + c0); float zz = z[row];
    for (int i = 0; i < 4; i++) { a[2 * i] += zz * lo(v[i]); a[2 * i + 1] += zz * hi(v[i]); } }
  for (int row = t; row < F; row += 1024) { u32x4 v = *(const u32x4*)(wd + (size_t)row * D + c0); float zz = z[HQ * DH + row];
    for (int i = 0; i < 4; i++) { bb[2 * i] += zz * lo(v[i]); bb[2 * i + 1] += zz * hi(v[i]); } }
  for (int i = 0; i < 8; i++) { for (int k = 1; k < 64; k <<= 1) { a[i] += __shfl_xor(a[i], k); bb[i] += __shfl_xor(bb[i], k); } }
  if ((t & 63) == 0) for (int i = 0; i < 8; i++) { red[t >> 6][i] = a[i]; red[t >> 6][8 + i] = bb[i]; }
  __syncthreads();
  if (t < 8) { float sa = 0, sb = 0; for (int w = 0; w < 16; w++) { sa += red[w][t]; sb += red[w][8 + t]; }
    float g = gs; sa = rbf(sa); sb = rbf(sb);
    out[c0 + t] = __float2bfloat16(rbf(g * sa) + rbf(rbf(1.f - g) * sb)); }
}

extern "C" void kernel_launch(void* const* d_in, const int* in_sizes, int n_in, void* d_out, int out_size, void* d_ws, size_t ws_size,
                              hipStream_t stream) {
  const unsigned short* x = (const unsigned short*)d_in[0];
  const unsigned short* kc = (const unsigned short*)d_in[1];
  const unsigned short* vc = (const unsigned short*)d_in[2];
  const unsigned short* wq = (const unsigned short*)d_in[3];
  const unsigned short* wo = (const unsigned short*)d_in[4];
  const unsigned short* wg = (const unsigned short*)d_in[6];
  const unsigned short* wu = (const unsigned short*)d_in[7];
  const unsigned short* wd = (const unsigned short*)d_in[8];
  const unsigned short* gw = (const unsigned short*)d_in[9];
  float* y = (float*)d_ws; float* part = y + 16384;
  k1<<<144, 256, 0, stream>>>(x, wq, wg, wu, y);
  k2<<<256, 512, 0, stream>>>(kc, vc, y, part);
  k3<<<80, 1024, 0, stream>>>(x, gw, wo, wd, y, part, (__hip_bfloat16*)d_out);
}
```

```cpp
#include <hip/hip_runtime.h>
#include <hip/hip_bf16.h>
#include <cstdint>
#include <utility>

typedef unsigned int u32;
typedef unsigned long long u64;
typedef __attribute__((ext_vector_type(4))) unsigned u32x4;
typedef __attribute__((ext_vector_type(2))) unsigned u32x2;
typedef __attribute__((ext_vector_type(4))) float f32x4;
typedef __attribute__((ext_vector_type(2))) float f32x2;
typedef __attribute__((ext_vector_type(2))) __bf16 bf16x2;
typedef __attribute__((ext_vector_type(8))) __bf16 bf16x8;
typedef __amdgpu_buffer_rsrc_t rsrc_t;


constexpr int WIN = 4;
constexpr int G1 = 12;
constexpr int B1 = 3;
constexpr int B2 = 1;
constexpr int VB = 3;
constexpr int TD = 4;
constexpr int SK = 32768, W = 4096;
constexpr int NT = 2, SC1 = 16;
constexpr u32 OOB = 0x80000000u;
constexpr u32 WS_E = 0;
constexpr u32 WS_Q = 4096;
constexpr u32 WS_G = WS_Q + 32 * 7680;
constexpr u32 WS_O = WS_G + 24 * 5120;
constexpr u32 WS_P = WS_O + 96 * 5120;
constexpr u32 WS_END = WS_P + 40 * 3072;
constexpr u32 SPIN_MAX = 1u << 16;
constexpr int K0 = 6, V0 = 30, T0 = 54, NS = 59;
constexpr int issued(int i) {
  int n = i + 1 + WIN;
  if (n > T0) n = n - TD > T0 ? n - TD : T0;
  if (i + 1 + WIN + VB >= T0 && n < T0) n = T0;
  return i >= T0 - 1 ? NS : n;
}

template <int B, int E, class F> __device__ __forceinline__ void sfor(F&& f) {
  if constexpr (B < E) { f(std::integral_constant<int, B>{}); sfor<B + 1, E>(f); }
}
__device__ __forceinline__ rsrc_t mkrs(const void* p, u32 bytes) { return __builtin_amdgcn_make_buffer_rsrc((void*)p, 0, bytes, 0x00020000); }
template <int AUX> __device__ __forceinline__ u32x4 ld128(rsrc_t r, u32 v, u32 s) { return __builtin_amdgcn_raw_buffer_load_b128(r, v, s, AUX); }
template <int AUX> __device__ __forceinline__ u32x2 ld64(rsrc_t r, u32 v, u32 s) { return __builtin_amdgcn_raw_buffer_load_b64(r, v, s, AUX); }
template <int AUX> __device__ __forceinline__ u32 ld32(rsrc_t r, u32 v, u32 s) { return __builtin_amdgcn_raw_buffer_load_b32(r, v, s, AUX); }
__device__ __forceinline__ void st_granules(rsrc_t r, u32 v, u32 s, float a, float b, u32 tag) {
  u32x4 g = {__float_as_uint(a), tag, __float_as_uint(b), tag};
  __builtin_amdgcn_raw_buffer_store_b128(g, r, v, s, SC1);
}
__device__ __forceinline__ float f0(u32x4 g) { return __uint_as_float(g[0]); }
__device__ __forceinline__ float f1(u32x4 g) { return __uint_as_float(g[2]); }
__device__ __forceinline__ u32 stale(u32x4 g, u32 tag) { return (g[1] ^ tag) | (g[3] ^ tag); }
__device__ __forceinline__ u32 pk_bf16(float a, float b) { f32x2 v = {a, b}; return __builtin_bit_cast(u32, __builtin_convertvector(v, bf16x2)); }
__device__ __forceinline__ float rbf(float a) { return __uint_as_float(pk_bf16(a, a) & 0xffff0000u); }
__device__ __forceinline__ float blo(u32 v) { return __uint_as_float(v << 16); }
__device__ __forceinline__ float bhi(u32 v) { return __uint_as_float(v & 0xffff0000u); }
template <int CTRL> __device__ __forceinline__ float dpp(float v) {
  return __uint_as_float(__builtin_amdgcn_update_dpp(0u, __float_as_uint(v), CTRL, 0xf, 0xf, true));
}
constexpr int QP1 = 0xB1, QP2 = 0x4E, HMIR = 0x141, ROR8 = 0x128;
__device__ __forceinline__ float sum8(float v) { v += dpp<QP1>(v); v += dpp<QP2>(v); v += dpp<HMIR>(v); return v; }
__device__ __forceinline__ float max8(float v) { v = fmaxf(v, dpp<QP1>(v)); v = fmaxf(v, dpp<QP2>(v)); v = fmaxf(v, dpp<HMIR>(v)); return v; }
__device__ __forceinline__ float sum_x16(float v) { auto r = __builtin_amdgcn_permlane16_swap(__float_as_uint(v), __float_as_uint(v), false, false); return __uint_as_float(r[0]) + __uint_as_float(r[1]); }
__device__ __forceinline__ float sum_x32(float v) { auto r = __builtin_amdgcn_permlane32_swap(__float_as_uint(v), __float_as_uint(v), false, false); return __uint_as_float(r[0]) + __uint_as_float(r[1]); }
__device__ __forceinline__ float max_x16(float v) { auto r = __builtin_amdgcn_permlane16_swap(__float_as_uint(v), __float_as_uint(v), false, false); return fmaxf(__uint_as_float(r[0]), __uint_as_float(r[1])); }
__device__ __forceinline__ float max_x32(float v) { auto r = __builtin_amdgcn_permlane32_swap(__float_as_uint(v), __float_as_uint(v), false, false); return fmaxf(__uint_as_float(r[0]), __uint_as_float(r[1])); }
__device__ __forceinline__ float sum_rows(float v) { v += dpp<ROR8>(v); return sum_x32(sum_x16(v)); }
__device__ __forceinline__ float max_rows(float v) { v = fmaxf(v, dpp<ROR8>(v)); return max_x32(max_x16(v)); }
__device__ __forceinline__ float dot2(u32 a, u32 b, float c) { return __builtin_amdgcn_fdot2_f32_bf16(__builtin_bit_cast(bf16x2, a), __builtin_bit_cast(bf16x2, b), c, false); }
__device__ __forceinline__ f32x4 mfma(u32x4 a, u32x4 b, f32x4 c) {
  return __builtin_amdgcn_mfma_f32_16x16x32_bf16(__builtin_bit_cast(bf16x8, a), __builtin_bit_cast(bf16x8, b), c, 0, 0, 0);
}
__device__ __forceinline__ u32x4 onehot(u32 zz, u32x4 mask) { u32x4 z = {zz, zz, zz, zz}; return z & mask; }
__device__ __forceinline__ void wg_barrier() { asm volatile("s_waitcnt lgkmcnt(0)\n\ts_barrier" ::: "memory"); }
__device__ __forceinline__ void fence_c() { asm volatile("" ::: "memory"); }
template <class T> __device__ __forceinline__ void arrive(const T& v) { asm volatile("" ::"v"(v)); }
#define SB() __builtin_amdgcn_sched_barrier(0)
template <class B, class G> __device__ __forceinline__ u32 spin(B&& bad, G&& gather) {
  u32 n = 0;
  if (__builtin_expect(bad() != 0, 0)) {
    do { fence_c(); gather(); ++n; } while (bad() != 0 && n < SPIN_MAX);
  }
  return n;
}
template <class T> __device__ __forceinline__ u32 lds_addr(T* p) { return (u32)(size_t)(__attribute__((address_space(3))) T*)p; }
__device__ __forceinline__ float fold16(float a, float b) { auto r = __builtin_amdgcn_permlane16_swap(__float_as_uint(a), __float_as_uint(b), false, false); return __uint_as_float(r[0]) + __uint_as_float(r[1]); }
__device__ __forceinline__ float fold32(float a, float b) { auto r = __builtin_amdgcn_permlane32_swap(__float_as_uint(a), __float_as_uint(b), false, false); return __uint_as_float(r[0]) + __uint_as_float(r[1]); }


struct Lds {
  float part1[16 * 192];
  float part2[16 * 192];
  float part3[16 * 64];
  unsigned short q[192];
  u32 hid[32];
  float g[8], l[8];
  u32 f1[8], fq[8];
  u32x2 fm[8];
  u32 tag;
  u32 pad[16384];
};

template <int LD> __device__ __forceinline__ f32x2 merge16(const float* p, int lane) {
  const float* a = p + (lane >> 5) * 8 * LD + (lane & 31) * 2;
  f32x2 v[8];
  for (int k = 0; k < 8; k++) v[k] = *(const f32x2*)(a + k * LD);
  for (int d = 4; d > 0; d >>= 1) for (int k = 0; k < d; k++) v[k] += v[k + d];
  f32x2 o = {sum_x32(v[0][0]), sum_x32(v[0][1])};
  return o;
}

__global__ void __launch_bounds__(512, 2)
fused_decode(const void* __restrict__ x, const void* __restrict__ kc, const void* __restrict__ vc, const void* __restrict__ wq,
             const void* __restrict__ wo, const void* __restrict__ wg, const void* __restrict__ wu, const void* __restrict__ wd,
             const void* __restrict__ gw, void* out, void* ws) {
  __shared__ __attribute__((aligned(16))) Lds L;
  const u32 b = blockIdx.x, grp = b & 7, j = b >> 3;
  const u32 h = grp * 4 + (j & 3), s = j >> 2;
  const bool proj = j < 30;
  const u32 j10 = (j * 26) >> 8, rho = grp * 3 + j10, ell = j - j10 * 10;
  const u32 t = threadIdx.x, lane = t & 63, w = __builtin_amdgcn_readfirstlane(t >> 6);
  const u32 r = lane >> 3, c = lane & 7, q4 = lane >> 4;
  const u32 ls = ell >= 5, kc5 = ell - ls * 5;
  const u32 row1 = kc5 * 128 + w * 16;
  u32 q_s = row1 * 12288 + rho * 512 + ls * 256, g_s = row1 * 3072 + rho * 128, q_v = r * 2 * 12288 + c * 16, g_v = r * 2 * 3072 + c * 16;
  u32 kv_s = (h * SK + (SK - W) + s * 512 + w * 64) * 384u, kv_v = r * 384 + c * 16;
  u32 nq = proj ? 640u * 12288u : 0u, ng = proj ? 640u * 3072u : 0u, x_v = r * 4, x_s = row1 * 2;
  asm volatile("" ::"s"(q_s), "s"(g_s), "s"(kv_s), "s"(nq), "s"(ng), "s"(x_s), "v"(q_v), "v"(g_v), "v"(kv_v), "v"(x_v));
  SB();
  asm volatile("" ::"s"(x), "s"(kc), "s"(vc), "s"(wq), "s"(wo), "s"(wg), "s"(wu), "s"(wd), "s"(gw), "s"(out), "s"(ws));
  const rsrc_t rk = mkrs(kc, 32u * SK * 384u), rv = mkrs(vc, 32u * SK * 384u), rw = mkrs(ws, WS_END);
  const rsrc_t rq = mkrs(wq, nq), rg = mkrs(ls ? wu : wg, ng), ro = mkrs(wo, nq), rd = mkrs(wd, ng);
  const rsrc_t rx = mkrs(x, 1280u), rgw = mkrs(gw, 1280u);

  u32x4 buf[NS];
  auto stream = [&](auto I) {
    constexpr int i = I;
    if constexpr (i < K0) {
      constexpr int a = i / 3, jj = i % 3;
      if constexpr (jj < 2) buf[i] = ld128<NT>(rq, q_v + jj * 128, q_s + a * 12288);
      else buf[i] = ld128<NT>(rg, g_v + a * 3072, g_s);
    } else if constexpr (i < T0) {
      constexpr int k = (i - K0) % 24;
      buf[i] = ld128<NT>(i < V0 ? rk : rv, kv_v + (k % 3) * 128, kv_s + (k / 3) * 3072);
    } else if constexpr (i == T0) {
      buf[i] = ld128<NT>(rd, r * 1280 + c * 16, (rho * 64 + w * 8) * 1280 + ell * 128);
    } else if constexpr (i < NS) {
      buf[i] = ld128<NT>(ro, (q4 * 8 + (r & 1)) * 1280 + c * 16, (rho * 256 + w * 32 + 2 * (i - T0 - 1)) * 1280 + ell * 128);
    }
    SB();
  };
  sfor<0, WIN>(stream);
  const u32 x2 = ld32<0>(rx, x_v, x_s);
  const u32 epoch = ld32<SC1>(rw, t == 0 ? WS_E : OOB, 0);
  SB();
  if (t < 32) L.f1[t] = 0;
  wg_barrier();

  u32x4 mask;
  { u32 half = (c & 1) ? 0xffff0000u : 0x0000ffffu;
    for (int k = 0; k < 4; k++) mask[k] = (c >> 1) == k ? half : 0u; }
  const bool dvalid = (q4 >> 1) == (r & 1);
  const u32 dsrc = 2 * w + (q4 >> 1), dcol = c * 8 + (q4 & 1) * 4;

  u32 tag;
  u32x4 ga[3], qv[3];
  u32 gx, gy;
  float sc[8], m, lsum;
  u32 pp[8];
  const f32x4 zero4 = {0, 0, 0, 0};
  f32x4 acc[3] = {zero4, zero4, zero4};
  const bool g1on = r < 5 && (w < 4 || proj);
  const u32 g1_v = g1on ? r * (w < 4 ? 1536 : 512) + c * 16 : OOB;
  const u32 g1_s = w < 4 ? WS_Q + h * 7680 + w * 384 : WS_G + rho * 5120 + (w - 4) * 128;
  auto gather1 = [&]() {
    ga[0] = ld128<SC1>(rw, g1_v, g1_s); ga[1] = ld128<SC1>(rw, g1_v, g1_s + (w < 4 ? 128 : 2560));
    ga[2] = ld128<SC1>(rw, g1_v, w < 4 ? g1_s + 256 : WS_END);
  };
  auto qk = [&](auto G) {
    constexpr int g = G;
    float a0 = 0.f, a1 = 0.f, a2 = 0.f;
    for (int e = 0; e < 4; e++) { a0 = dot2(buf[K0 + 3 * g][e], qv[0][e], a0); a1 = dot2(buf[K0 + 3 * g + 1][e], qv[1][e], a1); a2 = dot2(buf[K0 + 3 * g + 2][e], qv[2][e], a2); }
    sc[g] = rbf(rbf(sum8(a0 + a1 + a2)) * 0.07216878364870322f);
  };
  auto pv = [&](auto G) {
    constexpr int g = G;
    const u32x4 a = onehot(pp[g], mask);
    for (int k = 0; k < 3; k++) acc[k] = mfma(a, buf[V0 + 3 * g + k], acc[k]);
  };

  constexpr int C1 = K0 + G1 + 1, CQ = C1 + B2, CG = V0 + 4, CM = V0 + 12;
  sfor<0, T0>([&](auto I) {
    constexpr int i = I;
    arrive(buf[i]);
    SB();
    sfor<issued(i - 1), issued(i)>(stream);
    if constexpr (i + WIN == K0 + G1) gather1();
    if constexpr (i + WIN + 1 == CG) { gx = ld32<0>(rx, lane < 40 ? lane * 4 : OOB, w * 160); gy = ld32<0>(rgw, lane < 40 ? lane * 4 : OOB, w * 160); }
    SB();
    if constexpr (i == K0 - 1) {
      const u32x4 a0 = onehot(__builtin_amdgcn_perm(x2, x2, 0x01000100u), mask), a1 = onehot(__builtin_amdgcn_perm(x2, x2, 0x03020302u), mask);
      f32x4 a3[3];
      for (int k = 0; k < 3; k++) a3[k] = mfma(a1, buf[3 + k], mfma(a0, buf[k], zero4));
      if (dvalid) for (int k = 0; k < 3; k++) *(f32x4*)&L.part1[dsrc * 192 + k * 64 + dcol] = a3[k];
      if (t == 0) L.tag = epoch;
      fence_c();
      if (lane == 0) L.f1[w] = 1;
      fence_c();
    }
    if constexpr (i == K0 - 1 + B1) {
      u32 f, e;
      asm volatile(".Lp1_%=:\n\tds_read_b32 %0, %2\n\tds_read_b32 %1, %3\n\ts_waitcnt lgkmcnt(0)\n\tv_cmp_eq_u32 vcc, 0, %0\n\ts_cbranch_vccnz .Lp1_%="
                   : "=&v"(f), "=&v"(e) : "v"(lds_addr(&L.f1[c])), "v"(lds_addr(&L.tag)) : "vcc", "memory");
      tag = __builtin_amdgcn_readfirstlane(e) + 1;
      if (w < 3) {
        const f32x2 v = merge16<192>(L.part1 + w * 64, lane);
        const u32 qg64 = rho * 4 + ls * 2 + w, qh = (qg64 * 171) >> 9;
        const u32 dst = w < 2 ? WS_Q + qh * 7680 + kc5 * 1536 + (qg64 - qh * 3) * 512 : WS_G + rho * 5120 + ell * 512;
        st_granules(rw, (lane < 32 && proj) ? lane * 16 : OOB, dst, v[0], v[1], tag);
      }
    }
    if constexpr (i == C1) {
      spin([&]() { return g1on ? stale(ga[0], tag) | stale(ga[1], tag) | (w < 4 ? stale(ga[2], tag) : 0u) : 0u; }, gather1);
      float u = fold32(fold16(f0(ga[0]), f1(ga[0])), fold16(f0(ga[1]), f1(ga[1])));
      u += dpp<ROR8>(u);
      if (w < 4) {
        float v = sum_x32(fold16(f0(ga[2]), f1(ga[2])));
        v += dpp<ROR8>(v);
        const u32 pk = pk_bf16(u, v);
        if (!(r & 1) || r < 4) L.q[w * 48 + ((r & 1) ? 32 : (q4 >> 1) * 16) + c * 2 + (q4 & 1)] = (r & 1) ? pk >> 16 : pk;
        fence_c();
        if (lane == 0) L.fq[w] = 1;
        fence_c();
      } else {
        const float gt = rbf(u);
        const float up = __uint_as_float(__builtin_amdgcn_permlane32_swap(__float_as_uint(gt), __float_as_uint(gt), false, false)[1]);
        const float hv = rbf(gt * __builtin_amdgcn_rcpf(1.f + __expf(-1.5957691216057308f * (gt + 0.044715f * gt * gt * gt)))) * up;
        if (!(r & 1) && r < 4) ((unsigned short*)L.hid)[(w - 4) * 16 + c * 2 + q4] = pk_bf16(hv, hv);
      }
    }
    if constexpr (i == CQ) {
      u32 f;
      asm volatile(".Lpq_%=:\n\tds_read_b32 %0, %4\n\tds_read_b128 %1, %5\n\tds_read_b128 %2, %5 offset:128\n\tds_read_b128 %3, %5 offset:256\n\t"
                   "s_waitcnt lgkmcnt(0)\n\tv_cmp_eq_u32 vcc, 0, %0\n\ts_cbranch_vccnz .Lpq_%="
                   : "=&v"(f), "=&v"(qv[0]), "=&v"(qv[1]), "=&v"(qv[2]) : "v"(lds_addr(&L.fq[c & 3])), "v"(lds_addr(&L.q[c * 8])) : "vcc", "memory");
    }
    sfor<0, 8>([&](auto G) { constexpr int g = G, ready = K0 + 3 * g + 2; if constexpr (i == (CQ + g > ready ? CQ + g : ready)) qk(G); });
    if constexpr (i == V0) {
      m = sc[0];
      for (int g = 1; g < 8; g++) m = fmaxf(m, sc[g]);
      m = max_rows(m);
      if (lane == 0) { u32x2 v = {__float_as_uint(m), 1u}; L.fm[w] = v; }
      fence_c();
    }
    if constexpr (i == V0 + 1) {
      lsum = 0.f;
      for (int g = 0; g < 4; g++) { const float p = __expf(sc[g] - m); lsum += p; pp[g] = pk_bf16(p, p); }
    }
    if constexpr (i == CG) {
      const float gp = sum_rows(sum8(rbf(blo(gx) * blo(gy)) + rbf(bhi(gx) * bhi(gy))));
      if (lane == 0) L.g[w] = gp;
    }
    if constexpr (i == CM) {
      u32 fl; float mo;
      asm volatile(".Lpm_%=:\n\tds_read_b32 %0, %2 offset:4\n\tds_read_b32 %1, %2\n\ts_waitcnt lgkmcnt(0)\n\tv_cmp_eq_u32 vcc, 0, %0\n\ts_cbranch_vccnz .Lpm_%="
                   : "=&v"(fl), "=&v"(mo) : "v"(lds_addr(&L.fm[c])) : "vcc", "memory");
      const float mw = max8(mo), f = __expf(m - mw);
      m = mw;
      for (int k = 0; k < 3; k++) acc[k] *= f;
      lsum *= f;
      for (int g = 4; g < 8; g++) { const float p = __expf(sc[g] - m); lsum += p; pp[g] = pk_bf16(p, p); }
    }
    if constexpr (i == CM + 1) { lsum = sum_rows(lsum); if (lane == 0) L.l[w] = lsum; }
    if constexpr (i > V0 && (i - V0) % 3 == 2) pv(std::integral_constant<int, (i - V0) / 3>{});
    SB();
  });

  if (dvalid) for (int k = 0; k < 3; k++) *(f32x4*)&L.part2[dsrc * 192 + k * 64 + dcol] = acc[k];
  wg_barrier();
  if (w < 3) {
    const f32x4 l4 = *(const f32x4*)&L.l[0] + *(const f32x4*)&L.l[4];
    const f32x2 v = merge16<192>(L.part2 + w * 64, lane);
    st_granules(rw, lane < 40 ? lane * 16 : OOB, WS_O + (h * 3 + w) * 5120 + s * 640, lane < 32 ? v[0] : m, lane < 32 ? v[1] : (l4[0] + l4[1]) + (l4[2] + l4[3]), tag);
  }
  if (proj) {
    u32x4 oa, ob, ml;
    const u32 g3_v = r * 640 + c * 16, g3_s = WS_O + (rho * 4 + (w >> 1)) * 5120;
    auto gather3 = [&]() {
      oa = ld128<SC1>(rw, g3_v + (r & 1) * 128, g3_s + (w & 1) * 256); ob = ld128<SC1>(rw, g3_v + (~r & 1) * 128, g3_s + (w & 1) * 256);
      ml = ld128<SC1>(rw, g3_v + 512, g3_s);
    };
    gather3();
    SB();
    const f32x4 g03 = *(const f32x4*)&L.g[0], g47 = *(const f32x4*)&L.g[4];
    const float gs = rbf(((g03[0] + g03[1]) + (g03[2] + g03[3])) + ((g47[0] + g47[1]) + (g47[2] + g47[3])));
    const float gate = rbf(__builtin_amdgcn_rcpf(1.f + __expf(-gs)));
    const u32 hh = ((const unsigned short*)L.hid)[w * 8 + r];
    f32x4 ab = mfma(onehot(hh | (hh << 16), mask), buf[T0], zero4);
    ab *= rbf(1.f - gate);
    const float sel0 = (r & 1) ? 0.f : 1.f, sel1 = 1.f - sel0;
    const u32 p3 = dsrc * 64 + dcol;
    asm volatile("" ::"v"(ab), "v"(gate), "v"(sel0), "v"(sel1), "v"(p3));
    SB();
    spin([&]() { return stale(oa, tag) | stale(ob, tag) | stale(ml, tag); }, gather3);
    const float e = __expf(f0(ml) - max_rows(f0(ml))), inv = __builtin_amdgcn_rcpf(sum_rows(e * f1(ml)));
    f32x4 z = __builtin_amdgcn_mfma_f32_16x16x4f32(e * f0(oa) + dpp<ROR8>(e * f0(ob)), sel0, zero4, 0, 0, 0);
    z = __builtin_amdgcn_mfma_f32_16x16x4f32(e * f1(oa) + dpp<ROR8>(e * f1(ob)), sel1, z, 0, 0, 0);
    z *= inv;
    f32x4 aa = zero4;
    for (int k = 0; k < 4; k++) aa = mfma(onehot(pk_bf16(z[k], z[k]), mask), buf[T0 + 1 + k], aa);
    aa = aa * gate + ab;
    if (dvalid) *(f32x4*)&L.part3[p3] = aa;
    wg_barrier();
    if (w == 7) {
      const f32x2 v = merge16<64>(L.part3, lane);
      st_granules(rw, lane < 32 ? r * 3072 + c * 16 : OOB, WS_P + ell * 4 * 3072 + rho * 128, v[0], v[1], tag);
    }
  } else {
    const u32 unit = grp * 2 + (j - 30) + 16 * (7 - w);
    if (unit < 40) {
      u32x4 pa[3];
      auto gather4 = [&]() { for (int k = 0; k < 3; k++) pa[k] = ld128<SC1>(rw, lane * 16 + k * 1024, WS_P + unit * 3072); };
      gather4();
      spin([&]() { return stale(pa[0], tag) | stale(pa[1], tag) | stale(pa[2], tag); }, gather4);
      float v = sum_x32(fold16(f0(pa[0]) + f0(pa[1]) + f0(pa[2]), f1(pa[0]) + f1(pa[1]) + f1(pa[2])));
      v += dpp<ROR8>(v);
      if (!(r & 1) && r < 4) ((unsigned short*)out)[unit * 16 + c * 2 + q4] = pk_bf16(v, v);
      if (unit == 0) __builtin_amdgcn_raw_buffer_store_b32(tag, rw, lane == 0 ? WS_E : OOB, 0, SC1);
    }
  }
}

extern "C" void kernel_launch(void* const* d_in, const int* in_sizes, int n_in, void* d_out, int out_size, void* d_ws, size_t ws_size,
                              hipStream_t stream) {
  fused_decode<<<256, 512, 0, stream>>>(d_in[0], d_in[1], d_in[2], d_in[3], d_in[4], d_in[6], d_in[7], d_in[8], d_in[9], d_out, d_ws);
}
```

```cpp
#define VB 3
#define TD 4
#define DLY 0
#include <hip/hip_runtime.h>
#include <hip/hip_bf16.h>
#include <cstdint>
#include <utility>

typedef unsigned int u32;
typedef unsigned long long u64;
typedef __attribute__((ext_vector_type(4))) unsigned u32x4;
typedef __attribute__((ext_vector_type(2))) unsigned u32x2;
typedef __attribute__((ext_vector_type(4))) float f32x4;
typedef __attribute__((ext_vector_type(2))) float f32x2;
typedef __attribute__((ext_vector_type(2))) __bf16 bf16x2;
typedef __attribute__((ext_vector_type(8))) __bf16 bf16x8;
typedef __amdgpu_buffer_rsrc_t rsrc_t;

#define WIN 4
#define G1 12
#define B1 3
#define B2 1
#define XS 0
#define XG 0
constexpr int SK = 32768, W = 4096;
constexpr int NT = 2, SC1 = 16;
constexpr u32 OOB = 0x80000000u;
constexpr u32 WS_E = 0;
constexpr u32 WS_Q = 4096;
constexpr u32 WS_G = WS_Q + 32 * 7680;
constexpr u32 WS_O = WS_G + 24 * 5120;
constexpr u32 WS_P = WS_O + 96 * 5120;
constexpr u32 WS_END = WS_P + 40 * 3072;
constexpr u32 SPIN_MAX = 1u << 16;
constexpr int K0 = 6, V0 = 30, T0 = 54, NS = 59;
constexpr int issued(int i) {
  int n = i + 1 + WIN;
  if (n > T0) n = n - TD > T0 ? n - TD : T0;
  if (i + 1 + WIN + VB >= T0 && n < T0) n = T0;
  return i >= T0 - 1 ? NS : n;
}

template <int B, int E, class F> __device__ __forceinline__ void sfor(F&& f) {
  if constexpr (B < E) { f(std::integral_constant<int, B>{}); sfor<B + 1, E>(f); }
}
__device__ __forceinline__ rsrc_t mkrs(const void* p, u32 bytes) { return __builtin_amdgcn_make_buffer_rsrc((void*)p, 0, bytes, 0x00020000); }
template <int AUX> __device__ __forceinline__ u32x4 ld128(rsrc_t r, u32 v, u32 s) { return __builtin_amdgcn_raw_buffer_load_b128(r, v, s, AUX); }
template <int AUX> __device__ __forceinline__ u32x2 ld64(rsrc_t r, u32 v, u32 s) { return __builtin_amdgcn_raw_buffer_load_b64(r, v, s, AUX); }
template <int AUX> __device__ __forceinline__ u32 ld32(rsrc_t r, u32 v, u32 s) { return __builtin_amdgcn_raw_buffer_load_b32(r, v, s, AUX); }
__device__ __forceinline__ void st_granules(rsrc_t r, u32 v, u32 s, float a, float b, u32 tag) {
  u32x4 g = {__float_as_uint(a), tag, __float_as_uint(b), tag};
  __builtin_amdgcn_raw_buffer_store_b128(g, r, v, s, SC1);
}
__device__ __forceinline__ float f0(u32x4 g) { return __uint_as_float(g[0]); }
__device__ __forceinline__ float f1(u32x4 g) { return __uint_as_float(g[2]); }
__device__ __forceinline__ u32 stale(u32x4 g, u32 tag) { return (g[1] ^ tag) | (g[3] ^ tag); }
__device__ __forceinline__ u32 pk_bf16(float a, float b) { f32x2 v = {a, b}; return __builtin_bit_cast(u32, __builtin_convertvector(v, bf16x2)); }
__device__ __forceinline__ float rbf(float a) { return __uint_as_float(pk_bf16(a, a) & 0xffff0000u); }
__device__ __forceinline__ float blo(u32 v) { return __uint_as_float(v << 16); }
__device__ __forceinline__ float bhi(u32 v) { return __uint_as_float(v & 0xffff0000u); }
template <int CTRL> __device__ __forceinline__ float dpp(float v) {
  return __uint_as_float(__builtin_amdgcn_update_dpp(0u, __float_as_uint(v), CTRL, 0xf, 0xf, true));
}
constexpr int QP1 = 0xB1, QP2 = 0x4E, HMIR = 0x141, ROR8 = 0x128;
__device__ __forceinline__ float sum8(float v) { v += dpp<QP1>(v); v += dpp<QP2>(v); v += dpp<HMIR>(v); return v; }
__device__ __forceinline__ float max8(float v) { v = fmaxf(v, dpp<QP1>(v)); v = fmaxf(v, dpp<QP2>(v)); v = fmaxf(v, dpp<HMIR>(v)); return v; }
__device__ __forceinline__ float sum_x16(float v) { auto r = __builtin_amdgcn_permlane16_swap(__float_as_uint(v), __float_as_uint(v), false, false); return __uint_as_float(r[0]) + __uint_as_float(r[1]); }
__device__ __forceinline__ float sum_x32(float v) { auto r = __builtin_amdgcn_permlane32_swap(__float_as_uint(v), __float_as_uint(v), false, false); return __uint_as_float(r[0]) + __uint_as_float(r[1]); }
__device__ __forceinline__ float max_x16(float v) { auto r = __builtin_amdgcn_permlane16_swap(__float_as_uint(v), __float_as_uint(v), false, false); return fmaxf(__uint_as_float(r[0]), __uint_as_float(r[1])); }
__device__ __forceinline__ float max_x32(float v) { auto r = __builtin_amdgcn_permlane32_swap(__float_as_uint(v), __float_as_uint(v), false, false); return fmaxf(__uint_as_float(r[0]), __uint_as_float(r[1])); }
__device__ __forceinline__ float sum_rows(float v) { v += dpp<ROR8>(v); return sum_x32(sum_x16(v)); }
__device__ __forceinline__ float max_rows(float v) { v = fmaxf(v, dpp<ROR8>(v)); return max_x32(max_x16(v)); }
__device__ __forceinline__ float dot2(u32 a, u32 b, float c) { return __builtin_amdgcn_fdot2_f32_bf16(__builtin_bit_cast(bf16x2, a), __builtin_bit_cast(bf16x2, b), c, false); }
__device__ __forceinline__ f32x4 mfma(u32x4 a, u32x4 b, f32x4 c) {
  return __builtin_amdgcn_mfma_f32_16x16x32_bf16(__builtin_bit_cast(bf16x8, a), __builtin_bit_cast(bf16x8, b), c, 0, 0, 0);
}
__device__ __forceinline__ u32x4 onehot(u32 zz, u32x4 mask) { u32x4 z = {zz, zz, zz, zz}; return z & mask; }
__device__ __forceinline__ void wg_barrier() { asm volatile("s_waitcnt lgkmcnt(0)\n\ts_barrier" ::: "memory"); }
__device__ __forceinline__ void fence_c() { asm volatile("" ::: "memory"); }
template <class T> __device__ __forceinline__ void arrive(const T& v) { asm volatile("" ::"v"(v)); }
#define SB() __builtin_amdgcn_sched_barrier(0)
template <class B, class G> __device__ __forceinline__ u32 spin(B&& bad, G&& gather) {
  u32 n = 0;
  if (__builtin_expect(bad() != 0, 0)) {
    do { fence_c(); gather(); ++n; } while (bad() != 0 && n < SPIN_MAX);
  }
  return n;
}
template <class T> __device__ __forceinline__ u32 lds_addr(T* p) { return (u32)(size_t)(__attribute__((address_space(3))) T*)p; }
__device__ __forceinline__ float fold16(float a, float b) { auto r = __builtin_amdgcn_permlane16_swap(__float_as_uint(a), __float_as_uint(b), false, false); return __uint_as_float(r[0]) + __uint_as_float(r[1]); }
__device__ __forceinline__ float fold32(float a, float b) { auto r = __builtin_amdgcn_permlane32_swap(__float_as_uint(a), __float_as_uint(b), false, false); return __uint_as_float(r[0]) + __uint_as_float(r[1]); }

#define STAMP(i) do { if ((i) == DLY) { SB(); tdly = __builtin_amdgcn_s_memrealtime(); SB(); } } while (0)

struct Lds {
  float part1[16 * 192];
  float part2[16 * 192];
  float part3[16 * 64];
  unsigned short q[192];
  u32 hid[32];
  float g[8], l[8];
  u32 f1[8], fq[8];
  u32x2 fm[8];
  u32 tag;
  u32 pad[16384];
};

template <int LD> __device__ __forceinline__ f32x2 merge16(const float* p, int lane) {
  const float* a = p + (lane >> 5) * 8 * LD + (lane & 31) * 2;
  f32x2 v[8];
  for (int k = 0; k < 8; k++) v[k] = *(const f32x2*)(a + k * LD);
  for (int d = 4; d > 0; d >>= 1) for (int k = 0; k < d; k++) v[k] += v[k + d];
  f32x2 o = {sum_x32(v[0][0]), sum_x32(v[0][1])};
  return o;
}

__global__ void __launch_bounds__(512, 2)
fused_decode(const void* __restrict__ x, const void* __restrict__ kc, const void* __restrict__ vc, const void* __restrict__ wq,
             const void* __restrict__ wo, const void* __restrict__ wg, const void* __restrict__ wu, const void* __restrict__ wd,
             const void* __restrict__ gw, void* out, void* ws) {
  __shared__ __attribute__((aligned(16))) Lds L;
  u64 tdly = 0;
  const u32 b = blockIdx.x, grp = b & 7, j = b >> 3;
  const u32 h = grp * 4 + (j & 3), s = j >> 2;
  const bool proj = j < 30;
  const u32 j10 = (j * 26) >> 8, rho = grp * 3 + j10, ell = j - j10 * 10;
  const u32 t = threadIdx.x, lane = t & 63, w = __builtin_amdgcn_readfirstlane(t >> 6);
  const u32 r = lane >> 3, c = lane & 7, q4 = lane >> 4;
  const u32 ls = ell >= 5, kc5 = ell - ls * 5;
  const u32 row1 = kc5 * 128 + w * 16;
  u32 q_s = row1 * 12288 + rho * 512 + ls * 256, g_s = row1 * 3072 + rho * 128, q_v = r * 2 * 12288 + c * 16, g_v = r * 2 * 3072 + c * 16;
  u32 kv_s = (h * SK + (SK - W) + s * 512 + w * 64) * 384u, kv_v = r * 384 + c * 16;
  u32 nq = proj ? 640u * 12288u : 0u, ng = proj ? 640u * 3072u : 0u, x_v = r * 4, x_s = row1 * 2;
  asm volatile("" ::"s"(q_s), "s"(g_s), "s"(kv_s), "s"(nq), "s"(ng), "s"(x_s), "v"(q_v), "v"(g_v), "v"(kv_v), "v"(x_v));
  SB();
  asm volatile("" ::"s"(x), "s"(kc), "s"(vc), "s"(wq), "s"(wo), "s"(wg), "s"(wu), "s"(wd), "s"(gw), "s"(out), "s"(ws));
  STAMP(0);
  const rsrc_t rk = mkrs(kc, 32u * SK * 384u), rv = mkrs(vc, 32u * SK * 384u), rw = mkrs(ws, WS_END);
  const rsrc_t rq = mkrs(wq, nq), rg = mkrs(ls ? wu : wg, ng), ro = mkrs(wo, nq), rd = mkrs(wd, ng);
  const rsrc_t rx = mkrs(x, 1280u), rgw = mkrs(gw, 1280u);

  u32x4 buf[NS];
  auto stream = [&](auto I) {
    constexpr int i = I;
    if constexpr (i < K0) {
      constexpr int a = i / 3, jj = i % 3;
      if constexpr (jj < 2) buf[i] = ld128<NT>(rq, q_v + jj * 128, q_s + a * 12288);
      else buf[i] = ld128<NT>(rg, g_v + a * 3072, g_s);
    } else if constexpr (i < T0) {
      constexpr int k = (i - K0) % 24;
      buf[i] = ld128<NT>(i < V0 ? rk : rv, kv_v + (k % 3) * 128, kv_s + (k / 3) * 3072);
    } else if constexpr (i == T0) {
      buf[i] = ld128<NT>(rd, r * 1280 + c * 16, (rho * 64 + w * 8) * 1280 + ell * 128);
    } else if constexpr (i < NS) {
      buf[i] = ld128<NT>(ro, (q4 * 8 + (r & 1)) * 1280 + c * 16, (rho * 256 + w * 32 + 2 * (i - T0 - 1)) * 1280 + ell * 128);
    }
    SB();
  };
  sfor<0, WIN>(stream);
  const u32 x2 = ld32<0>(rx, x_v, x_s);
  const u32 epoch = ld32<SC1>(rw, t == 0 ? WS_E : OOB, 0);
  SB();
  if (t < 32) L.f1[t] = 0;
  wg_barrier();

  u32x4 mask;
  { u32 half = (c & 1) ? 0xffff0000u : 0x0000ffffu;
    for (int k = 0; k < 4; k++) mask[k] = (c >> 1) == k ? half : 0u; }
  const bool dvalid = (q4 >> 1) == (r & 1);
  const u32 dsrc = 2 * w + (q4 >> 1), dcol = c * 8 + (q4 & 1) * 4;

  u32 tag, nsp[3] = {0, 0, 0};
  u32x4 ga[3], qv[3];
  u32 gx, gy;
  float sc[8], m, lsum;
  u32 pp[8];
  const f32x4 zero4 = {0, 0, 0, 0};
  f32x4 acc[3] = {zero4, zero4, zero4};
  const bool g1on = r < 5 && (w < 4 || proj);
  const u32 g1_v = g1on ? r * (w < 4 ? 1536 : 512) + c * 16 : OOB;
  const u32 g1_s = w < 4 ? WS_Q + h * 7680 + w * 384 : WS_G + rho * 5120 + (w - 4) * 128;
  auto gather1 = [&]() {
    ga[0] = ld128<SC1>(rw, g1_v, g1_s); ga[1] = ld128<SC1>(rw, g1_v, g1_s + (w < 4 ? 128 : 2560));
    ga[2] = ld128<SC1>(rw, g1_v, w < 4 ? g1_s + 256 : WS_END);
  };
  auto qk = [&](auto G) {
    constexpr int g = G;
    float a0 = 0.f, a1 = 0.f, a2 = 0.f;
    for (int e = 0; e < 4; e++) { a0 = dot2(buf[K0 + 3 * g][e], qv[0][e], a0); a1 = dot2(buf[K0 + 3 * g + 1][e], qv[1][e], a1); a2 = dot2(buf[K0 + 3 * g + 2][e], qv[2][e], a2); }
    sc[g] = rbf(rbf(sum8(a0 + a1 + a2)) * 0.07216878364870322f);
  };
  auto pv = [&](auto G) {
    constexpr int g = G;
    const u32x4 a = onehot(pp[g], mask);
    for (int k = 0; k < 3; k++) acc[k] = mfma(a, buf[V0 + 3 * g + k], acc[k]);
  };

  constexpr int C1 = K0 + G1 + 1, CQ = C1 + B2, CG = V0 + 4, CM = V0 + 12;
  sfor<0, T0>([&](auto I) {
    constexpr int i = I;
    arrive(buf[i]);
    SB();
    sfor<issued(i - 1), issued(i)>(stream);
    if constexpr (i + WIN == K0 + G1) { if (XG & 1) { ga[0] = mask; ga[1] = mask; ga[2] = mask; } else gather1(); }
    if constexpr (i + WIN + 1 == CG) { gx = ld32<0>(rx, lane < 40 ? lane * 4 : OOB, w * 160); gy = ld32<0>(rgw, lane < 40 ? lane * 4 : OOB, w * 160); }
    SB();
    if constexpr (i == K0 - 1) {
      const u32x4 a0 = onehot(__builtin_amdgcn_perm(x2, x2, 0x01000100u), mask), a1 = onehot(__builtin_amdgcn_perm(x2, x2, 0x03020302u), mask);
      f32x4 a3[3];
      for (int k = 0; k < 3; k++) a3[k] = mfma(a1, buf[3 + k], mfma(a0, buf[k], zero4));
      if (dvalid) for (int k = 0; k < 3; k++) *(f32x4*)&L.part1[dsrc * 192 + k * 64 + dcol] = a3[k];
      if (t == 0) L.tag = epoch;
      fence_c();
      if (lane == 0) L.f1[w] = 1;
      fence_c();
    }
    if constexpr (i == K0 - 1 + B1) {
      u32 f, e;
      asm volatile(".Lp1_%=:\n\tds_read_b32 %0, %2\n\tds_read_b32 %1, %3\n\ts_waitcnt lgkmcnt(0)\n\tv_cmp_eq_u32 vcc, 0, %0\n\ts_cbranch_vccnz .Lp1_%="
                   : "=&v"(f), "=&v"(e) : "v"(lds_addr(&L.f1[c])), "v"(lds_addr(&L.tag)) : "vcc", "memory");
      tag = __builtin_amdgcn_readfirstlane(e) + 1;
      if (w < 3) {
        const f32x2 v = merge16<192>(L.part1 + w * 64, lane);
        const u32 qg64 = rho * 4 + ls * 2 + w, qh = (qg64 * 171) >> 9;
        const u32 dst = w < 2 ? WS_Q + qh * 7680 + kc5 * 1536 + (qg64 - qh * 3) * 512 : WS_G + rho * 5120 + ell * 512;
        if (!(XS & 1) || v[0] == 1.2345f) st_granules(rw, (lane < 32 && proj) ? lane * 16 : OOB, dst, v[0], v[1], tag);
      }
      STAMP(1);
    }
    if constexpr (i == C1) {
      if (!((XS | XG) & 1)) nsp[0] = spin([&]() { return g1on ? stale(ga[0], tag) | stale(ga[1], tag) | (w < 4 ? stale(ga[2], tag) : 0u) : 0u; }, gather1);
      float u = fold32(fold16(f0(ga[0]), f1(ga[0])), fold16(f0(ga[1]), f1(ga[1])));
      u += dpp<ROR8>(u);
      if (w < 4) {
        float v = sum_x32(fold16(f0(ga[2]), f1(ga[2])));
        v += dpp<ROR8>(v);
        const u32 pk = pk_bf16(u, v);
        if (!(r & 1) || r < 4) L.q[w * 48 + ((r & 1) ? 32 : (q4 >> 1) * 16) + c * 2 + (q4 & 1)] = (r & 1) ? pk >> 16 : pk;
        fence_c();
        if (lane == 0) L.fq[w] = 1;
        fence_c();
      } else {
        const float gt = rbf(u);
        const float up = __uint_as_float(__builtin_amdgcn_permlane32_swap(__float_as_uint(gt), __float_as_uint(gt), false, false)[1]);
        const float hv = rbf(gt * __builtin_amdgcn_rcpf(1.f + __expf(-1.5957691216057308f * (gt + 0.044715f * gt * gt * gt)))) * up;
        if (!(r & 1) && r < 4) ((unsigned short*)L.hid)[(w - 4) * 16 + c * 2 + q4] = pk_bf16(hv, hv);
      }
    }
    if constexpr (i == CQ) {
      u32 f;
      asm volatile(".Lpq_%=:\n\tds_read_b32 %0, %4\n\tds_read_b128 %1, %5\n\tds_read_b128 %2, %5 offset:128\n\tds_read_b128 %3, %5 offset:256\n\t"
                   "s_waitcnt lgkmcnt(0)\n\tv_cmp_eq_u32 vcc, 0, %0\n\ts_cbranch_vccnz .Lpq_%="
                   : "=&v"(f), "=&v"(qv[0]), "=&v"(qv[1]), "=&v"(qv[2]) : "v"(lds_addr(&L.fq[c & 3])), "v"(lds_addr(&L.q[c * 8])) : "vcc", "memory");
      STAMP(2);
    }
    sfor<0, 8>([&](auto G) { constexpr int g = G, ready = K0 + 3 * g + 2; if constexpr (i == (CQ + g > ready ? CQ + g : ready)) qk(G); });
    if constexpr (i == V0) {
      m = sc[0];
      for (int g = 1; g < 8; g++) m = fmaxf(m, sc[g]);
      m = max_rows(m);
      if (lane == 0) { u32x2 v = {__float_as_uint(m), 1u}; L.fm[w] = v; }
      fence_c();
      STAMP(3);
    }
    if constexpr (i == V0 + 1) {
      lsum = 0.f;
      for (int g = 0; g < 4; g++) { const float p = __expf(sc[g] - m); lsum += p; pp[g] = pk_bf16(p, p); }
    }
    if constexpr (i == CG) {
      const float gp = sum_rows(sum8(rbf(blo(gx) * blo(gy)) + rbf(bhi(gx) * bhi(gy))));
      if (lane == 0) L.g[w] = gp;
    }
    if constexpr (i == CM) {
      u32 fl; float mo;
      asm volatile(".Lpm_%=:\n\tds_read_b32 %0, %2 offset:4\n\tds_read_b32 %1, %2\n\ts_waitcnt lgkmcnt(0)\n\tv_cmp_eq_u32 vcc, 0, %0\n\ts_cbranch_vccnz .Lpm_%="
                   : "=&v"(fl), "=&v"(mo) : "v"(lds_addr(&L.fm[c])) : "vcc", "memory");
      const float mw = max8(mo), f = __expf(m - mw);
      m = mw;
      for (int k = 0; k < 3; k++) acc[k] *= f;
      lsum *= f;
      for (int g = 4; g < 8; g++) { const float p = __expf(sc[g] - m); lsum += p; pp[g] = pk_bf16(p, p); }
      STAMP(4);
    }
    if constexpr (i == CM + 1) { lsum = sum_rows(lsum); if (lane == 0) L.l[w] = lsum; }
    if constexpr (i > V0 && (i - V0) % 3 == 2) pv(std::integral_constant<int, (i - V0) / 3>{});
    SB();
  });
  STAMP(5);

  if (dvalid) for (int k = 0; k < 3; k++) *(f32x4*)&L.part2[dsrc * 192 + k * 64 + dcol] = acc[k];
  wg_barrier();
  if (w < 3) {
    const f32x4 l4 = *(const f32x4*)&L.l[0] + *(const f32x4*)&L.l[4];
    const f32x2 v = merge16<192>(L.part2 + w * 64, lane);
    st_granules(rw, lane < 40 ? lane * 16 : OOB, WS_O + (h * 3 + w) * 5120 + s * 640, lane < 32 ? v[0] : m, lane < 32 ? v[1] : (l4[0] + l4[1]) + (l4[2] + l4[3]), tag);
  }
  STAMP(6);
  if (proj) {
    u32x4 oa, ob, ml;
    const u32 g3_v = r * 640 + c * 16, g3_s = WS_O + (rho * 4 + (w >> 1)) * 5120;
    auto gather3 = [&]() {
      oa = ld128<SC1>(rw, g3_v + (r & 1) * 128, g3_s + (w & 1) * 256); ob = ld128<SC1>(rw, g3_v + (~r & 1) * 128, g3_s + (w & 1) * 256);
      ml = ld128<SC1>(rw, g3_v + 512, g3_s);
    };
    gather3();
    SB();
    const f32x4 g03 = *(const f32x4*)&L.g[0], g47 = *(const f32x4*)&L.g[4];
    const float gs = rbf(((g03[0] + g03[1]) + (g03[2] + g03[3])) + ((g47[0] + g47[1]) + (g47[2] + g47[3])));
    const float gate = rbf(__builtin_amdgcn_rcpf(1.f + __expf(-gs)));
    const u32 hh = ((const unsigned short*)L.hid)[w * 8 + r];
    f32x4 ab = mfma(onehot(hh | (hh << 16), mask), buf[T0], zero4);
    ab *= rbf(1.f - gate);
    const float sel0 = (r & 1) ? 0.f : 1.f, sel1 = 1.f - sel0;
    const u32 p3 = dsrc * 64 + dcol;
    asm volatile("" ::"v"(ab), "v"(gate), "v"(sel0), "v"(sel1), "v"(p3));
    SB();
    nsp[2] = spin([&]() { return stale(oa, tag) | stale(ob, tag) | stale(ml, tag); }, gather3);
    STAMP(7);
    const float e = __expf(f0(ml) - max_rows(f0(ml))), inv = __builtin_amdgcn_rcpf(sum_rows(e * f1(ml)));
    f32x4 z = __builtin_amdgcn_mfma_f32_16x16x4f32(e * f0(oa) + dpp<ROR8>(e * f0(ob)), sel0, zero4, 0, 0, 0);
    z = __builtin_amdgcn_mfma_f32_16x16x4f32(e * f1(oa) + dpp<ROR8>(e * f1(ob)), sel1, z, 0, 0, 0);
    z *= inv;
    f32x4 aa = zero4;
    for (int k = 0; k < 4; k++) aa = mfma(onehot(pk_bf16(z[k], z[k]), mask), buf[T0 + 1 + k], aa);
    aa = aa * gate + ab;
    if (dvalid) *(f32x4*)&L.part3[p3] = aa;
    wg_barrier();
    if (w == 7) {
      const f32x2 v = merge16<64>(L.part3, lane);
      st_granules(rw, lane < 32 ? r * 3072 + c * 16 : OOB, WS_P + ell * 4 * 3072 + rho * 128, v[0], v[1], tag);
    }
    STAMP(8);
  } else {
    const u32 unit = grp * 2 + (j - 30) + 16 * (7 - w);
    if (unit < 40) {
      u32x4 pa[3];
      auto gather4 = [&]() { for (int k = 0; k < 3; k++) pa[k] = ld128<SC1>(rw, lane * 16 + k * 1024, WS_P + unit * 3072); };
      gather4();
      spin([&]() { return stale(pa[0], tag) | stale(pa[1], tag) | stale(pa[2], tag); }, gather4);
      float v = sum_x32(fold16(f0(pa[0]) + f0(pa[1]) + f0(pa[2]), f1(pa[0]) + f1(pa[1]) + f1(pa[2])));
      v += dpp<ROR8>(v);
      if (!(r & 1) && r < 4) ((unsigned short*)out)[unit * 16 + c * 2 + q4] = pk_bf16(v, v);
      if (unit == 0) __builtin_amdgcn_raw_buffer_store_b32(tag, rw, lane == 0 ? WS_E : OOB, 0, SC1);
    }
    STAMP(7); STAMP(8);
  }
  while (__builtin_amdgcn_s_memrealtime() < tdly + 4000) __builtin_amdgcn_s_sleep(2);
}

extern "C" void kernel_launch(void* const* d_in, const int* in_sizes, int n_in, void* d_out, int out_size, void* d_ws, size_t ws_size,
                              hipStream_t stream) {
  fused_decode<<<256, 512, 0, stream>>>(d_in[0], d_in[1], d_in[2], d_in[3], d_in[4], d_in[6], d_in[7], d_in[8], d_in[9], d_out, d_ws);
}
```

```cpp
#include <hip/hip_runtime.h>
#include <hip/hip_bf16.h>
#include <cstdint>
#include <utility>

typedef unsigned int u32;
typedef unsigned long long u64;
typedef __attribute__((ext_vector_type(4))) unsigned u32x4;
typedef __attribute__((ext_vector_type(2))) unsigned u32x2;
typedef __attribute__((ext_vector_type(8))) unsigned long long u64x8;
typedef __attribute__((ext_vector_type(2))) unsigned long long u64x2;
typedef __attribute__((ext_vector_type(4))) float f32x4;
typedef __attribute__((ext_vector_type(2))) float f32x2;
typedef __attribute__((ext_vector_type(2))) __bf16 bf16x2;
typedef __attribute__((ext_vector_type(8))) __bf16 bf16x8;
typedef __amdgpu_buffer_rsrc_t rsrc_t;


constexpr int SK = 32768, W = 4096;
constexpr int NT = 2, SC1 = 16;
constexpr u32 OOB = 0x80000000u;
constexpr u32 WS_E = 0;
constexpr u32 TSTEP_ = 0x01030507u;
constexpr u32 SPIN_MAX = 1u << 16;
constexpr int K0 = 6, V0 = 30, T0 = 54, NS = 59;
constexpr int issued(int i, int WIN, int TW) {
  int n = i + 1 + WIN;
  if (n > T0) n = i + 1 + TW > T0 ? i + 1 + TW : T0;
  return i >= T0 - 1 || n > NS ? NS : n;
}

template <int B, int E, class F> __device__ __forceinline__ void sfor(F&& f) {
  if constexpr (B < E) { f(std::integral_constant<int, B>{}); sfor<B + 1, E>(f); }
}
__device__ __forceinline__ rsrc_t mkrs(const void* p, u32 bytes) { return __builtin_amdgcn_make_buffer_rsrc((void*)p, 0, bytes, 0x00020000); }
template <int AUX> __device__ __forceinline__ u32x4 ld128(rsrc_t r, u32 v, u32 s) { return __builtin_amdgcn_raw_buffer_load_b128(r, v, s, AUX); }
template <int AUX> __device__ __forceinline__ u32x2 ld64(rsrc_t r, u32 v, u32 s) { return __builtin_amdgcn_raw_buffer_load_b64(r, v, s, AUX); }
template <int AUX> __device__ __forceinline__ u32 ld32(rsrc_t r, u32 v, u32 s) { return __builtin_amdgcn_raw_buffer_load_b32(r, v, s, AUX); }
__device__ __forceinline__ void st_word(rsrc_t r, u32 v, u32 s, u32 x) { __builtin_amdgcn_raw_buffer_store_b32(x, r, v, s, SC1); }
__device__ __forceinline__ void st_quad(rsrc_t r, u32 v, u32 s, u32x4 x) { __builtin_amdgcn_raw_buffer_store_b128(x, r, v, s, SC1); }
__device__ __forceinline__ u32x4 rows4(u32 v) {
  auto r = __builtin_amdgcn_permlane32_swap(v, v, false, false);
  auto a = __builtin_amdgcn_permlane16_swap(r[0], r[0], false, false), b = __builtin_amdgcn_permlane16_swap(r[1], r[1], false, false);
  u32x4 x = {a[0], a[1], b[0], b[1]};
  return x;
}
__device__ __forceinline__ u32 tagged(float a, u32 tagb) { return (__float_as_uint(a) & 0xffffff00u) | tagb; }
__device__ __forceinline__ float val(u32 x) { return __uint_as_float(x & 0xffffff00u); }
__device__ __forceinline__ float mid(u32 x) { return __uint_as_float((x & 0xffffff00u) | 0x80u); }
__device__ __forceinline__ u32 tag_of(u32x4 g) { return __builtin_amdgcn_perm(g[1], g[0], 0x0c0c0400u) | __builtin_amdgcn_perm(g[3], g[2], 0x04000c0cu); }
__device__ __forceinline__ u32 pk_bf16(float a, float b) { f32x2 v = {a, b}; return __builtin_bit_cast(u32, __builtin_convertvector(v, bf16x2)); }
__device__ __forceinline__ float rbf(float a) { return __uint_as_float(pk_bf16(a, a) & 0xffff0000u); }
__device__ __forceinline__ float blo(u32 v) { return __uint_as_float(v << 16); }
__device__ __forceinline__ float bhi(u32 v) { return __uint_as_float(v & 0xffff0000u); }
template <int CTRL> __device__ __forceinline__ float dpp(float v) {
  return __uint_as_float(__builtin_amdgcn_update_dpp(0u, __float_as_uint(v), CTRL, 0xf, 0xf, true));
}
constexpr int QP1 = 0xB1, QP2 = 0x4E, HMIR = 0x141, ROR4 = 0x124, ROR8 = 0x128;
template <int CTRL> __device__ __forceinline__ u32 dppu(u32 v) { return __builtin_amdgcn_update_dpp(0u, v, CTRL, 0xf, 0xf, true); }
__device__ __forceinline__ float sum8(float v) { v += dpp<QP1>(v); v += dpp<QP2>(v); v += dpp<HMIR>(v); return v; }
__device__ __forceinline__ float max8(float v) { v = fmaxf(v, dpp<QP1>(v)); v = fmaxf(v, dpp<QP2>(v)); v = fmaxf(v, dpp<HMIR>(v)); return v; }
__device__ __forceinline__ float sum_x16(float v) { auto r = __builtin_amdgcn_permlane16_swap(__float_as_uint(v), __float_as_uint(v), false, false); return __uint_as_float(r[0]) + __uint_as_float(r[1]); }
__device__ __forceinline__ float sum_x32(float v) { auto r = __builtin_amdgcn_permlane32_swap(__float_as_uint(v), __float_as_uint(v), false, false); return __uint_as_float(r[0]) + __uint_as_float(r[1]); }
__device__ __forceinline__ float max_x16(float v) { auto r = __builtin_amdgcn_permlane16_swap(__float_as_uint(v), __float_as_uint(v), false, false); return fmaxf(__uint_as_float(r[0]), __uint_as_float(r[1])); }
__device__ __forceinline__ float max_x32(float v) { auto r = __builtin_amdgcn_permlane32_swap(__float_as_uint(v), __float_as_uint(v), false, false); return fmaxf(__uint_as_float(r[0]), __uint_as_float(r[1])); }
__device__ __forceinline__ float sum_rows(float v) { v += dpp<ROR8>(v); return sum_x32(sum_x16(v)); }
__device__ __forceinline__ float max_rows(float v) { v = fmaxf(v, dpp<ROR8>(v)); return max_x32(max_x16(v)); }
__device__ __forceinline__ float dot2(u32 a, u32 b, float c) { return __builtin_amdgcn_fdot2_f32_bf16(__builtin_bit_cast(bf16x2, a), __builtin_bit_cast(bf16x2, b), c, false); }
__device__ __forceinline__ f32x4 mfma(u32x4 a, u32x4 b, f32x4 c) {
  return __builtin_amdgcn_mfma_f32_16x16x32_bf16(__builtin_bit_cast(bf16x8, a), __builtin_bit_cast(bf16x8, b), c, 0, 0, 0);
}
__device__ __forceinline__ u32x4 onehot(u32 zz, u32x4 mask) { u32x4 z = {zz, zz, zz, zz}; return z & mask; }
__device__ __forceinline__ void wg_barrier() { asm volatile("s_waitcnt lgkmcnt(0)\n\ts_barrier" ::: "memory"); }
__device__ __forceinline__ void fence_c() { asm volatile("" ::: "memory"); }
template <class T> __device__ __forceinline__ void arrive(const T& v) { asm volatile("" ::"v"(v)); }
#define SB() __builtin_amdgcn_sched_barrier(0)
__device__ __forceinline__ void pace(u32 due) {
  asm volatile(".Lw_%=:\n\ts_memrealtime vcc\n\ts_waitcnt lgkmcnt(0)\n\ts_lshl_b32 vcc_lo, vcc_lo, 4\n\ts_sub_u32 vcc_lo, vcc_lo, %0\n\ts_cmp_ge_i32 vcc_lo, 0\n\t"
               "s_cbranch_scc1 .Le_%=\n\ts_sleep 1\n\ts_branch .Lw_%=\n.Le_%=:" ::"s"(due) : "scc", "vcc");
}
template <class B, class G> __device__ __forceinline__ u32 spin(B&& bad, G&& gather) {
  u32 n = 0;
  if (__builtin_expect(bad(), 0)) {
    do { fence_c(); gather(); ++n; } while (bad() && n < SPIN_MAX);
  }
  return n;
}
template <class B, class G> __device__ __forceinline__ u32 gather_spin(B&& bad, G&& gather) {
  u32 n = 0;
  do { fence_c(); gather(); ++n; } while (bad() && n < SPIN_MAX);
  return n - 1;
}
template <class T> __device__ __forceinline__ u32 lds_addr(T* p) { return (u32)(size_t)(__attribute__((address_space(3))) T*)p; }
__device__ __forceinline__ float poll2(u32x2* p) {
  u32 f; float v;
  asm volatile(".Lpm_%=:\n\tds_read_b32 %0, %2 offset:4\n\tds_read_b32 %1, %2\n\ts_waitcnt lgkmcnt(0)\n\tv_cmp_eq_u32 vcc, 0, %0\n\ts_cbranch_vccnz .Lpm_%="
               : "=&v"(f), "=&v"(v) : "v"(lds_addr(p)) : "vcc", "memory");
  return v;
}
__device__ __forceinline__ float fold16(float a, float b) { auto r = __builtin_amdgcn_permlane16_swap(__float_as_uint(a), __float_as_uint(b), false, false); return __uint_as_float(r[0]) + __uint_as_float(r[1]); }
__device__ __forceinline__ float fold32(float a, float b) { auto r = __builtin_amdgcn_permlane32_swap(__float_as_uint(a), __float_as_uint(b), false, false); return __uint_as_float(r[0]) + __uint_as_float(r[1]); }
__device__ __forceinline__ float fold4(f32x4 v) { return fold32(fold16(v[0], v[1]), fold16(v[2], v[3])); }
__device__ __forceinline__ float fold4(u32x4 g) { f32x4 v = {val(g[0]), val(g[1]), val(g[2]), val(g[3])}; return fold4(v); }


#define STOP(m, k) do { if constexpr (HOP & (m)) { SB(); asm volatile(".Lstop%0:\n\ts_cbranch_execz .Lstop%1" ::"n"(k), "n"((k) + 1)); SB(); } \
  else if constexpr (HOP) asm volatile(".set .Lstop%0, .Lstop%1" ::"n"(k), "n"((k) + 1)); } while (0)
constexpr int NSTOP = 22;
struct Lds {
  float part1[16 * 192];
  float part2[16 * 192];
  float part3[16 * 64];
  unsigned short qh[256];
  u32x4 fin;
  u32 f1[8];
  u32x2 fm[8];
  u32 fq, ff;
  u32 pad[16384];
};

template <int LD> __device__ __forceinline__ u32 merge_at(u32 lane) { return ((lane >> 4) * 4 * LD + (lane & 15) * 4) * 4; }
template <int LD> __device__ __forceinline__ float merge16(const float* p, u32 at) {
  const float* a = (const float*)((const char*)p + at);
  return fold4((*(const f32x4*)a + *(const f32x4*)(a + LD)) + (*(const f32x4*)(a + 2 * LD) + *(const f32x4*)(a + 3 * LD)));
}

constexpr int WIN = 3;
constexpr int TW = 4;
constexpr int CQ = 22;
constexpr int PF = 3;
constexpr int XS = 3;
constexpr int C2 = 47;
constexpr int PRE = 0;
constexpr int TG = 16;
constexpr int HOP = 0;
constexpr int QR = 1;
constexpr int TP = 0;
constexpr int WIN2 = 5;
constexpr int SW = 8;
constexpr int KA = 1;
constexpr int TB = 16;
constexpr int AB = 1;
constexpr u32 tau = 500;
constexpr u32 tau1 = 500;
constexpr u32 lead = 700;
constexpr u32 od = 2800;
constexpr u32 pd = 4800;
constexpr u32 tau2 = 470;
constexpr u32 syn = 1;
constexpr u32 gd = 8000;
constexpr u32 md = 18700;
constexpr u32 taub = 440;
__global__ void __launch_bounds__(576, 3)
fused_decode(const void* x_, const void* kc_, const void* vc_, const void* wq_, const void* wo_, const void* wg_, const void* wu_, const void* wd_,
             const void* gw_, void* out_, void* ws_) {
  u64x8 ka; u64x2 kb; u64 kw, tw;
  if constexpr (KA) asm volatile("s_load_dwordx16 s[76:91], %0, 0\n\ts_load_dwordx4 s[92:95], %0, 0x40\n\ts_load_dwordx2 s[96:97], %0, 0x50\n\ts_memrealtime s[98:99]"
               ::"s"(__builtin_amdgcn_kernarg_segment_ptr()) : "s76", "s77", "s78", "s79", "s80", "s81", "s82", "s83", "s84", "s85", "s86", "s87", "s88", "s89", "s90", "s91", "s92", "s93", "s94", "s95", "s96", "s97", "s98", "s99");
  else tw = __builtin_amdgcn_s_memrealtime();
  if constexpr (KA) SB();
  constexpr u32 UQ = 256, UO = 256, UG = 256, UP = 256, UT = 128, UM = 128;
  constexpr u32 WS_T = 4096, WS_M = WS_T + 256 * UT, WS_Q = WS_M + 256 * UM, WS_G = WS_Q + 288 * UQ, WS_O = WS_G + 96 * UG, WS_P = WS_O + 768 * UO, WS_END = WS_P + 240 * UP;
  __shared__ __attribute__((aligned(16))) Lds L;
  const u32 b = blockIdx.x, grp = b & 7, j = b >> 3;
  const u32 h = grp * 4 + (j & 3), s = j >> 2;
  const bool proj = j < 30;
  const u32 j10 = (j * 26) >> 8, rho = ((grp + XS) & 7) * 3 + j10, ell = j - j10 * 10;
  const u32 t = threadIdx.x, lane = t & 63, w = __builtin_amdgcn_readfirstlane(t >> 6);
  u32 pf = 0;
  u64 pc; u32 len;
  if constexpr (PF > 0) {
    asm volatile("s_getpc_b64 %0\n.Lpf_pc%=:\n\ts_mov_b32 %1, .Lfused_end-.Lpf_pc%=" : "=s"(pc), "=s"(len));
    pf = ld32<0>(mkrs((const void*)pc, (PF == 1 || j >= 30) ? len : 0u), t * 128, 0);
    SB();
  }
  const u32 r = lane >> 3, c = lane & 7, q4 = lane >> 4;
  const u32 ls = ell >= 5, kc5 = ell - ls * 5;
  u32 s1_s[3], s1_d[3], s1_v[3];
  for (int a = 0; a < 3; a++) {
    const u32 n = kc5 * 3 + a, line = (n * 52) >> 8, row = (n - line * 5) * 128 + w * 16;
    s1_d[a] = line < 2 ? 12288 : 3072;
    s1_s[a] = row * s1_d[a] + (line < 2 ? (AB == 0 ? rho * 4 + ls * 2 + line : AB == 1 ? rho * 4 + ls + line * 2 : rho * 2 + ls + line * 48) * 128 : rho * 128);
    s1_v[a] = r * 2 * s1_d[a] + c * 16;
  }
  u32 kv_s = (h * SK + (SK - W) + (s * 8 + w) * 8) * 384u, kv_v = r * 384 + c * 16;
  u32 nq = proj && w < 8 ? 640u * 12288u : 0u, ng = proj ? 640u * 3072u : 0u, xn = kc5 * 3 + (c & 3), x_v = (xn - 5 * ((xn * 52) >> 8)) * 256 + r * 4;
  u32 n10 = kc5 < 4 ? nq : nq >> 2;
  asm volatile("" : "+s"(n10) : "s"(s1_s[0]), "s"(kv_s), "s"(nq), "s"(ng), "v"(s1_v[0]), "v"(s1_v[1]), "v"(kv_v), "v"(x_v));
  SB();
  if constexpr (KA) asm volatile("s_waitcnt lgkmcnt(0)\n\ts_cmp_gt_u32 %4, 4\n\ts_cselect_b64 s[86:87], s[88:89], s[86:87]\n\ts_cmp_lt_u32 %5, 4\n\ts_cselect_b64 s[88:89], s[82:83], s[86:87]"
               : "=&{s[76:91]}"(ka), "=&{s[92:95]}"(kb), "=&{s[96:97]}"(kw), "=&{s[98:99]}"(tw) : "s"(ell), "s"(kc5) : "scc");
  else {
    asm volatile("" ::"s"(x_), "s"(kc_), "s"(vc_), "s"(wq_), "s"(wo_), "s"(wg_), "s"(wu_), "s"(wd_), "s"(gw_), "s"(out_), "s"(ws_), "s"(tw));
  }
  const void *x = KA ? (void*)ka[0] : x_, *kc = KA ? (void*)ka[1] : kc_, *vc = KA ? (void*)ka[2] : vc_, *wq = KA ? (void*)ka[3] : wq_, *wo = KA ? (void*)ka[4] : wo_, *wg = KA ? (void*)ka[5] : wg_, *wu = KA ? (void*)ka[6] : wu_, *wd = KA ? (void*)ka[7] : wd_;
  const void *gw = KA ? (void*)kb[0] : gw_, *out = KA ? (void*)kb[1] : out_, *ws = KA ? (void*)kw : ws_;
  const u32 t0 = (u32)tw;
  const void *wgu = KA ? wg : ls ? wu : wg, *w10 = KA ? wu : kc5 < 4 ? wq : wgu;
  const rsrc_t rw = mkrs(ws, WS_END), rx = mkrs(x, 1280u), r10 = mkrs(w10, n10);
  const u32x4 first = ld128<NT>(r10, s1_v[0], s1_s[0]);
  SB();
  const u32 x2 = ld32<0>(rx, x_v, w * 32);
  const u32 epoch = ld32<SC1>(rw, WS_E, w == 8 ? 0 : WS_END);
  SB();
  asm volatile("s_cmp_lg_u32 %0, 8\n\ts_cbranch_scc1 .Lcl_%=\n\tds_write_b32 %1, %2\n\ts_waitcnt lgkmcnt(0)\n\ts_barrier\n.Lcl_%=:"
               ::"s"(w), "v"(lds_addr(L.f1) + lane * 4), "v"(0u) : "scc", "memory");
  const u32 o_s = WS_O + (h * 24 + s) * UO, p_s = WS_P + (ell * 24 + rho) * UP;
  const u32 pub_v = lane < 16 ? lane * 16 : OOB;

  if (w == 8) {
  const rsrc_t rgw = mkrs(gw, 1280u);
  const u32x4 gx = ld128<0>(rx, lane * 16, 0), gy = ld128<0>(rgw, lane * 16, 0);
  const u32 gx1 = ld32<0>(rx, lane * 4, 1024), gy1 = ld32<0>(rgw, lane * 4, 1024);
  const u32 tp_s = (h * SK + (SK - W) + s * 64) * 384u, tp_v = lane < 8 ? lane * (512 * 384) : OOB;
  u32 tp = 0;
  auto touch = [&](auto M) {
    constexpr int m = M;
    if constexpr (m & 1) tp |= ld32<0>(mkrs(kc, 32u * SK * 384u), lane < 1 ? 0 : OOB, tp_s);
    if constexpr (m & 2) tp |= ld32<0>(mkrs(kc, 32u * SK * 384u), tp_v, tp_s);
    if constexpr (m & 4) tp |= ld32<0>(mkrs(vc, 32u * SK * 384u), tp_v, tp_s);
    if constexpr (m & 8) { tp |= ld32<0>(mkrs(wo, ng * 4), lane < 8 ? lane * 32 * 1280 : OOB, rho * 256 * 1280 + ell * 128);
                           tp |= ld32<0>(mkrs(wd, ng), lane < 2 ? lane * 32 * 1280 : OOB, rho * 64 * 1280 + ell * 128); }
  };
  touch(std::integral_constant<int, TP & 15>{});
  SB();
  arrive(pf); arrive(first); arrive(x2);
  if constexpr (TP >> 4) { arrive(epoch); SB(); touch(std::integral_constant<int, (TP >> 4)>{}); SB(); }
  const u32 tag = __builtin_amdgcn_readfirstlane(epoch) + TSTEP_, tagb = (tag >> q4 * 8) & 0xffu;
  const u32 qa = WS_Q + (AB == 0 ? rho * 4 + ls * 2 : AB == 1 ? rho * 4 + ls : rho * 2 + ls) * 3 * UQ, qb = qa + (AB == 0 ? 1 : AB == 1 ? 2 : 48) * 3 * UQ, gu = WS_G + (rho * 2 + ls) * 2 * UG;
  const u32 dst0 = !proj ? WS_END : kc5 < 2 ? qa + kc5 * UQ : kc5 < 4 ? qb + (kc5 - 1) * UQ : gu + UG;
  const u32 dst1 = !proj ? WS_END : kc5 == 1 ? qb : kc5 == 3 ? gu : WS_END;
  const u32 m_s = WS_M + h * 8 * UM, half = lane < 8 ? lane * 16 : OOB;
  const u32x4 tag4 = rows4(tagb), wrong = ~tag4, tt = {t0, tag, tag, tag};
  st_quad(rw, half, TG < 0 || j == TG ? WS_T + (j * 8 + grp) * UT : WS_END, tt);
  float gp = rbf(blo(gx1) * blo(gy1)) + rbf(bhi(gx1) * bhi(gy1));
  for (int k = 0; k < 4; k++) gp += rbf(blo(gx[k]) * blo(gy[k])) + rbf(bhi(gx[k]) * bhi(gy[k]));
  const float gate = rbf(__builtin_amdgcn_rcpf(1.f + __expf(-rbf(sum_rows(sum8(gp))))));
  { u32 f;
    asm volatile(".Lp1_%=:\n\ts_sleep 1\n\tds_read_b32 %0, %1\n\ts_waitcnt lgkmcnt(0)\n\tv_cmp_eq_u32 vcc, 0, %0\n\ts_cbranch_vccnz .Lp1_%="
                 : "=&v"(f) : "v"(lds_addr(&L.f1[c])) : "vcc", "memory"); }
  { const float m0 = merge16<192>(L.part1, merge_at<192>(lane)), m1 = merge16<192>(L.part1, merge_at<192>(lane) + 256), m2 = merge16<192>(L.part1, merge_at<192>(lane) + 512);
    st_quad(rw, pub_v, dst0, rows4(tagged(m0 + (kc5 == 3 ? 0.f : m1) + (kc5 & 1 ? 0.f : m2), tagb)));
    st_quad(rw, pub_v, dst1, rows4(tagged(m2 + (kc5 == 3 ? m1 : 0.f), tagb))); }
  arrive(tp);
  const bool lq = lane < 48, third = !lq || (AB == 2 ? h >= 16 : ((h * 3 + q4) & (AB + 1)) != 0);
  const u32 g1_v = (lq ? WS_Q + (h * 3 + q4) * 3 * UQ : proj ? WS_G + rho * 4 * UG : OOB) + (lane & 15) * 16;
  const u32 gv[4] = {g1_v, g1_v, third ? g1_v : OOB, lq ? OOB : g1_v};
  const u32 want0 = lq || proj ? tag : 0u, want[4] = {want0, want0, third ? want0 : 0u, lq ? 0u : want0};
  u32x4 ga[4];
  u32x2 tb;
  pace((t0 << 4) + gd);
  tb = ld64<SC1>(rw, c * UT, WS_T + (TG < 0 ? j : TG) * 8 * UT);
  gather_spin([&]() { u32 bad = 0; for (int k = 0; k < 4; k++) bad |= tag_of(ga[k]) ^ want[k]; return bad != 0; },
                       [&]() { for (int k = 0; k < 4; k++) ga[k] = ld128<SC1>(rw, gv[k], k * UQ); });
  float y[4];
  for (int k = 0; k < 4; k++) {
    const float u = rbf(mid(ga[0][k]) + mid(ga[1][k]) + (lq ? mid(ga[2][k]) : 0.f));
    const float v = rbf(mid(ga[2][k]) + mid(ga[3][k]));
    const float hv = rbf(u * __builtin_amdgcn_rcpf(1.f + __expf(-1.5957691216057308f * (u + 0.044715f * u * u * u)))) * v;
    y[k] = lq ? u : hv;
  }
  { u32x2 v = {pk_bf16(y[0], y[1]), pk_bf16(y[2], y[3])}; *(u32x2*)&L.qh[lane * 4] = v; }
  int dt = tb[1] == tag ? min(max((int)(tb[0] - t0), -64), 64) : 0;
  dt += dppu<QP1>(dt); dt += dppu<QP2>(dt); dt += dppu<HMIR>(dt);
  const u32 base = ((t0 << 4) + __builtin_amdgcn_readfirstlane(dt) * 2 - syn) | 1u;
  fence_c();
  if (lane == 0) L.fq = base;
  fence_c();
  u32x2 mw;
  asm volatile(".Lpm_%=:\n\ts_sleep 2\n\tds_read_b64 %0, %1\n\ts_waitcnt lgkmcnt(0)\n\tv_cmp_gt_u64 vcc, %2, %0\n\ts_cbranch_vccnz .Lpm_%="
               : "=&v"(mw) : "v"(lds_addr(&L.fm[c])), "s"(1ull << 32) : "vcc", "memory");
  { u32x4 v = {mw[0] & 0xffffff00u, mw[1] & 0xffffff00u, 0u, 0u}; st_quad(rw, half, m_s + s * UM, v | tag4); }
  u32x4 ml;
  if constexpr (PF > 2) { const rsrc_t rc = mkrs((const void*)pc, (PF == 4 || j >= 30) ? len : 0u); u32 a = 0; for (int k = 0; k < 3; k++) a |= ld32<0>(rc, lane * 128, k * 8192); arrive(a); }
  pace(base + md);
  gather_spin([&]() { return tag_of(ml) != tag; }, [&]() { ml = ld128<SC1>(rw, r * UM + c * 16, m_s); });
  const float mh = max_rows(max8(bhi(ml[0])));
  const float lh = sum_rows(sum8(__expf(bhi(ml[0]) - mh) * mid(ml[1])));
  if (lane == 0) { u32x4 v = {__float_as_uint(mh), __float_as_uint(gate * __builtin_amdgcn_rcpf(lh)), __float_as_uint(rbf(1.f - gate)), tag}; L.fin = v; }
  fence_c();
  if (lane == 0) L.ff = 1;
  fence_c();
  if constexpr (PRE & 128) { st_quad(rw, pub_v, proj ? p_s : WS_END, wrong); asm volatile("s_waitcnt vmcnt(0)"); }
  if constexpr (HOP) asm volatile("s_mov_b64 exec, 0\n\ts_branch .Lstop0\n.Lstop%0:\n\ts_mov_b64 exec, -1" ::"n"(NSTOP));
  } else {
  const rsrc_t rk = mkrs(kc, 32u * SK * 384u), rv = mkrs(vc, 32u * SK * 384u);
  const rsrc_t r11 = mkrs(kc5 < 3 ? wq : wgu, kc5 < 3 ? nq : ng), ro = mkrs(wo, nq), rd = mkrs(wd, ng);
  u32x4 buf[NS];
  u32 due = (t0 << 4) - lead + w * taub / 8;
  auto stream = [&](auto I) {
    constexpr int i = I;
    if constexpr (i > 0 && i < T0) { due += i <= K0 ? tau1 : i < TB ? tau : taub; pace(due); SB(); }
    if constexpr (i >= T0 && i < T0 + TW - 1) { pace(due + (i + 1 - T0) * tau2); SB(); }
    if constexpr (i < K0) {
      constexpr int a = i % 3;
      buf[i] = ld128<NT>(a ? r11 : r10, s1_v[a ? 1 : 0], s1_s[a] + (i / 3) * s1_d[a]);
    } else if constexpr (i < T0) {
      constexpr int k = (i - K0) % 24;
      buf[i] = ld128<NT>(i < V0 ? rk : rv, kv_v + (k % 3) * 128, kv_s + (k / 3) * (512 * 384));
    } else if constexpr (i == T0) {
      buf[i] = ld128<NT>(rd, r * 1280 + c * 16, (rho * 64 + w * 8) * 1280 + ell * 128);
    } else if constexpr (i < NS) {
      buf[i] = ld128<NT>(ro, (q4 + (r & 1) * 16) * 1280 + c * 16, (rho * 256 + w * 32 + 4 * (i - T0 - 1)) * 1280 + ell * 128);
    }
    SB();
  };
  buf[0] = first;
  sfor<1, WIN>(stream);
  wg_barrier();

  u32x4 mask;
  { u32 half = (c & 1) ? 0xffff0000u : 0x0000ffffu;
    for (int k = 0; k < 4; k++) mask[k] = (c >> 1) == k ? half : 0u; }
  const bool dvalid = (q4 >> 1) == (r & 1);
  const u32 dsrc = 2 * w + (q4 >> 1), dcol = c * 8 + (q4 & 1) * 4;

  u32 tag;
  u32x4 qv[3], a7;
  u32 tagb, at2, pw2, o_w;
  float sc[8], m, p7, scale, rest;
  u32 pp[7];
  const f32x4 zero4 = {0, 0, 0, 0};
  f32x4 acc[3] = {zero4, zero4, zero4};
  auto qk = [&](auto G) {
    constexpr int g = G;
    float a0 = 0.f, a1 = 0.f, a2 = 0.f;
    for (int e = 0; e < 4; e++) { a0 = dot2(buf[K0 + 3 * g][e], qv[0][e], a0); a1 = dot2(buf[K0 + 3 * g + 1][e], qv[1][e], a1); a2 = dot2(buf[K0 + 3 * g + 2][e], qv[2][e], a2); }
    sc[g] = rbf(rbf(sum8(a0 + a1 + a2)) * 0.07216878364870322f);
  };
  auto pv = [&](auto G) {
    constexpr int g = G;
    const u32x4 a = onehot(pp[g], mask);
    for (int k = 0; k < 3; k++) acc[k] = mfma(a, buf[V0 + 3 * g + k], acc[k]);
  };

  constexpr int CX = CQ + 7 / QR + 1 > V0 ? CQ + 7 / QR + 1 : V0;
  sfor<0, T0>([&](auto I) {
    constexpr int i = I;
    arrive(buf[i]);
    if constexpr (i == 0) arrive(pf);
    if constexpr (i == 1) arrive(epoch);
    SB();
    if constexpr (i >= T0 - 3) {
      const f32x4 o = mfma(a7, buf[i], acc[i - (T0 - 3)]);
      if (dvalid) *(f32x4*)((char*)L.part2 + pw2 + (i - (T0 - 3)) * 256) = o;
      SB();
      STOP(8, 19 + i - (T0 - 3));
    }
    sfor<issued(i - 1, i - 1 < SW ? WIN : WIN2, TW), issued(i, i < SW ? WIN : WIN2, TW)>(stream);
    if constexpr (i == K0 - 1) {
      f32x4 a3[3];
      const u32 xe = __builtin_amdgcn_perm(x2, x2, 0x01000100u), xo = __builtin_amdgcn_perm(x2, x2, 0x03020302u);
      sfor<0, 3>([&](auto K) { constexpr int k = K; a3[k] = mfma(onehot(dppu<k * 0x55>(xo), mask), buf[3 + k], mfma(onehot(dppu<k * 0x55>(xe), mask), buf[k], zero4)); });
      if (dvalid) for (int k = 0; k < 3; k++) *(f32x4*)&L.part1[dsrc * 192 + k * 64 + dcol] = a3[k];
      fence_c();
      if (lane == 0) L.f1[w] = 1;
      fence_c();
    }
    if constexpr (i == CQ) {
      u32 f;
      asm volatile(".Lpq_%=:\n\tds_read_b32 %0, %4\n\tds_read_b128 %1, %5\n\tds_read_b128 %2, %5 offset:128\n\tds_read_b128 %3, %5 offset:256\n\t"
                   "s_waitcnt lgkmcnt(0)\n\tv_cmp_eq_u32 vcc, 0, %0\n\ts_cbranch_vccnz .Lpq_%="
                   : "=&v"(f), "=&v"(qv[0]), "=&v"(qv[1]), "=&v"(qv[2]) : "v"(lds_addr(&L.fq)), "v"(lds_addr(&L.qh[c * 8])) : "vcc", "memory");
      if (syn) due += __builtin_amdgcn_readfirstlane(f) - (t0 << 4);
    }
    sfor<0, 8>([&](auto G) { constexpr int g = G, ready = K0 + 3 * g + 2; if constexpr (i == (CQ + g / QR > ready ? CQ + g / QR : ready)) qk(G); });
    if constexpr (i == CX) {
      m = sc[0];
      for (int g = 1; g < 8; g++) m = fmaxf(m, sc[g]);
      m = max_rows(m);
      p7 = __expf(sc[7] - m);
      float lsum = p7;
      for (int g = 0; g < 7; g++) { const float p = __expf(sc[g] - m); lsum += p; pp[g] = pk_bf16(p, p); }
      lsum = sum_rows(lsum);
      if (lane == 0) { u32x2 v = {__float_as_uint(m), __float_as_uint(lsum)}; L.fm[w] = v; }
      fence_c();
    }
    if constexpr (i == C2) {
      u32 f; u32x4 v;
      asm volatile(".Lpz_%=:\n\tds_read_b32 %0, %2\n\tds_read_b128 %1, %3\n\ts_waitcnt lgkmcnt(0)\n\tv_cmp_eq_u32 vcc, 0, %0\n\ts_cbranch_vccnz .Lpz_%="
                   : "=&v"(f), "=&v"(v) : "v"(lds_addr(&L.ff)), "v"(lds_addr(&L.fin)) : "vcc", "memory");
      scale = __expf(m - __uint_as_float(v[0])) * __uint_as_float(v[1]);
      rest = __uint_as_float(v[2]);
      tag = __builtin_amdgcn_readfirstlane(v[3]);
      tagb = (tag >> q4 * 8) & 0xffu;
      a7 = onehot(pk_bf16(p7 * scale, p7 * scale), mask);
      at2 = merge_at<192>(lane) + w * 256;
      pw2 = (dsrc * 192 + dcol) * 4;
      o_w = o_s + w * 8 * UO;
      asm volatile("" : "+v"(scale), "+v"(rest), "+v"(at2), "+v"(pw2), "+v"(tagb), "+v"(a7), "+s"(o_w));
    }
    sfor<0, 7>([&](auto G) { constexpr int g = G, ready = V0 + 3 * g + 2; if constexpr (i == (CX + 1 + g > ready ? CX + 1 + g : ready)) pv(G); });
    if constexpr (i == T0 - 4) { for (int k = 0; k < 3; k++) { acc[k] *= scale; asm volatile("" : "+v"(acc[k])); } }
    SB();
  });
  auto publish = [&](auto K, auto LD_, const float* p, u32 at, u32 dst) {
    constexpr int k = K, LD = LD_;
    const float* a = (const float*)((const char*)p + at);
    STOP(1, k);
    const f32x4 a0 = *(const f32x4*)a, a1 = *(const f32x4*)(a + LD), a2 = *(const f32x4*)(a + 2 * LD), a3 = *(const f32x4*)(a + 3 * LD);
    STOP(2, k + 1);
    const f32x4 v = (a0 + a1) + (a2 + a3);
    STOP(1, k + 2);
    const u32 y = tagged(fold4(v), tagb);
    STOP(2, k + 3);
    st_quad(rw, pub_v, dst, rows4(y));
  };
  wg_barrier();
  if (w < 3) publish(std::integral_constant<int, 0>{}, std::integral_constant<int, 192>{}, L.part2, at2, o_w);
  if (proj) {
    u32x4 oa, mask3;
    u32 g3_v = r * UO + c * 16, g3_s = WS_O + (rho * 4 + (w >> 1)) * 8 * UO + (w & 1) * 128;
    auto gather3 = [&]() { oa = ld128<SC1>(rw, g3_v, g3_s); };
    for (int k = 0; k < 4; k++) mask3[k] = (r & 1) * 2 + ((c >> 1) & 1) == k ? (c & 1) ? 0xffff0000u : 0x0000ffffu : 0u;
    const u32 hh = L.qh[192 + w * 8 + q4 * 2 + (c >> 2)];
    u32 p3 = ((2 * w + (q4 & 1)) * 64 + c * 8 + (q4 >> 1) * 4) * 4, at3 = merge_at<64>(lane);
    const u32x4 ah = onehot(hh | (hh << 16), mask3);
    const bool keep3 = (q4 & 1) == (r & 1);
    u32 p_w = p_s;
    asm volatile("" : "+v"(p3), "+v"(at3), "+v"(g3_v), "+s"(p_w) : "v"(ah), "s"(g3_s), "s"(__builtin_amdgcn_ballot_w64(keep3)));
    SB();
    f32x4 aa = mfma(ah, buf[T0], zero4);
    aa *= rest;
    asm volatile("" ::"v"(aa));
    SB();
    pace(due - w * taub / 8 + od);
    gather_spin([&]() { return tag_of(oa) != tag; }, gather3);
    STOP(1, 4);
    float z = fold4(oa);
    STOP(2, 5);
    z += dpp<ROR8>(z);
    const u32 zz = pk_bf16(z, z);
    sfor<0, 4>([&](auto K) { constexpr int k = K; aa = mfma(onehot(dppu<k * 0x55>(zz), mask3), buf[T0 + 1 + k], aa); STOP(k & 1 ? 2 : 1, 6 + k); });
    if (keep3) *(f32x4*)((char*)L.part3 + p3) = aa;
    wg_barrier();
    if (w == 7) publish(std::integral_constant<int, 10>{}, std::integral_constant<int, 64>{}, L.part3, at3, p_w);
    STOP(4, 14);
  } else {
    const u32 unit = (grp * 2 + (j - 30)) * 5 + w - 3;
    if (w >= 3) {
      u32x4 pa;
      const u32 want = lane < 48 ? tag : 0u;
      auto gather4 = [&]() { pa = ld128<SC1>(rw, lane < 48 ? (lane >> 1) * UP + (lane & 1) * 16 : OOB, WS_P + (unit >> 3) * 24 * UP + (unit & 7) * 32); };
      const u32 o_v = (lane & 14) == 0 ? ((lane & 1) * 4 + q4) * 2 : OOB, oc = unit * 16;
      const rsrc_t rout = mkrs(out, 1280u);
      asm volatile("" ::"v"(o_v), "s"(oc), "v"(want), "s"(rout));
      pace(due - w * taub / 8 + pd);
      gather_spin([&]() { return tag_of(pa) != want; }, gather4);
      STOP(1, 15);
      float v = fold4(pa);
      STOP(2, 16);
      v += dpp<QP2>(v); v += dpp<ROR4>(v); v += dpp<ROR8>(v);
      STOP(1, 17);
      __builtin_amdgcn_raw_buffer_store_b16((unsigned short)pk_bf16(v, v), rout, o_v, oc, 0);
      STOP(4, 18);
      if (unit == 0) st_word(rw, lane == 0 ? WS_E : OOB, 0, tag);
    }
  }
  }
  asm volatile(".Lfused_end:");
}


extern "C" void kernel_launch(void* const* d_in, const int* in_sizes, int n_in, void* d_out, int out_size, void* d_ws, size_t ws_size,
                              hipStream_t stream) {
  fused_decode<<<256, 576, 0, stream>>>(d_in[0], d_in[1], d_in[2], d_in[3], d_in[4], d_in[6], d_in[7], d_in[8], d_in[9], d_out, d_ws);
}
```

```cpp
#include <hip/hip_runtime.h>
#include <hip/hip_bf16.h>
#include <cstdint>
#include <utility>

typedef unsigned int u32;
typedef unsigned long long u64;
typedef __attribute__((ext_vector_type(4))) unsigned u32x4;
typedef __attribute__((ext_vector_type(2))) unsigned u32x2;
typedef __attribute__((ext_vector_type(8))) unsigned long long u64x8;
typedef __attribute__((ext_vector_type(2))) unsigned long long u64x2;
typedef __attribute__((ext_vector_type(4))) float f32x4;
typedef __attribute__((ext_vector_type(2))) float f32x2;
typedef __attribute__((ext_vector_type(2))) __bf16 bf16x2;
typedef __attribute__((ext_vector_type(8))) __bf16 bf16x8;
typedef __amdgpu_buffer_rsrc_t rsrc_t;

constexpr int SK = 32768, W = 4096;
constexpr int NT = 2, SC1 = 16;
constexpr u32 OOB = 0x80000000u;
constexpr u32 WS_E = 0;
constexpr u32 TSTEP_ = 0x01030507u;
constexpr u32 SPIN_MAX = 1u << 16;
constexpr int K0 = 6, V0 = 30, T0 = 54, NS = 59;
constexpr int issued(int i, int WIN, int TW) {
  int n = i + 1 + WIN;
  if (n > T0) n = i + 1 + TW > T0 ? i + 1 + TW : T0;
  return i >= T0 - 1 || n > NS ? NS : n;
}

template <int B, int E, class F> __device__ __forceinline__ void sfor(F&& f) {
  if constexpr (B < E) { f(std::integral_constant<int, B>{}); sfor<B + 1, E>(f); }
}
__device__ __forceinline__ rsrc_t mkrs(const void* p, u32 bytes) { return __builtin_amdgcn_make_buffer_rsrc((void*)p, 0, bytes, 0x00020000); }
template <int AUX> __device__ __forceinline__ u32x4 ld128(rsrc_t r, u32 v, u32 s) { return __builtin_amdgcn_raw_buffer_load_b128(r, v, s, AUX); }
template <int AUX> __device__ __forceinline__ u32x2 ld64(rsrc_t r, u32 v, u32 s) { return __builtin_amdgcn_raw_buffer_load_b64(r, v, s, AUX); }
template <int AUX> __device__ __forceinline__ u32 ld32(rsrc_t r, u32 v, u32 s) { return __builtin_amdgcn_raw_buffer_load_b32(r, v, s, AUX); }
__device__ __forceinline__ void st_word(rsrc_t r, u32 v, u32 s, u32 x) { __builtin_amdgcn_raw_buffer_store_b32(x, r, v, s, SC1); }
__device__ __forceinline__ void st_quad(rsrc_t r, u32 v, u32 s, u32x4 x) { __builtin_amdgcn_raw_buffer_store_b128(x, r, v, s, SC1); }
__device__ __forceinline__ u32x4 rows4(u32 v) {
  auto r = __builtin_amdgcn_permlane32_swap(v, v, false, false);
  auto a = __builtin_amdgcn_permlane16_swap(r[0], r[0], false, false), b = __builtin_amdgcn_permlane16_swap(r[1], r[1], false, false);
  u32x4 x = {a[0], a[1], b[0], b[1]};
  return x;
}
__device__ __forceinline__ u32 tagged(float a, u32 tagb) { return (__float_as_uint(a) & 0xffffff00u) | tagb; }
__device__ __forceinline__ float val(u32 x) { return __uint_as_float(x & 0xffffff00u); }
__device__ __forceinline__ float mid(u32 x) { return __uint_as_float((x & 0xffffff00u) | 0x80u); }
__device__ __forceinline__ u32 tag_of(u32x4 g) { return __builtin_amdgcn_perm(g[1], g[0], 0x0c0c0400u) | __builtin_amdgcn_perm(g[3], g[2], 0x04000c0cu); }
__device__ __forceinline__ u32 pk_bf16(float a, float b) { f32x2 v = {a, b}; return __builtin_bit_cast(u32, __builtin_convertvector(v, bf16x2)); }
__device__ __forceinline__ float rbf(float a) { return __uint_as_float(pk_bf16(a, a) & 0xffff0000u); }
__device__ __forceinline__ float blo(u32 v) { return __uint_as_float(v << 16); }
__device__ __forceinline__ float bhi(u32 v) { return __uint_as_float(v & 0xffff0000u); }
template <int CTRL> __device__ __forceinline__ float dpp(float v) {
  return __uint_as_float(__builtin_amdgcn_update_dpp(0u, __float_as_uint(v), CTRL, 0xf, 0xf, true));
}
constexpr int QP1 = 0xB1, QP2 = 0x4E, HMIR = 0x141, ROR4 = 0x124, ROR8 = 0x128;
template <int CTRL> __device__ __forceinline__ u32 dppu(u32 v) { return __builtin_amdgcn_update_dpp(0u, v, CTRL, 0xf, 0xf, true); }
__device__ __forceinline__ float sum8(float v) { v += dpp<QP1>(v); v += dpp<QP2>(v); v += dpp<HMIR>(v); return v; }
__device__ __forceinline__ float max8(float v) { v = fmaxf(v, dpp<QP1>(v)); v = fmaxf(v, dpp<QP2>(v)); v = fmaxf(v, dpp<HMIR>(v)); return v; }
__device__ __forceinline__ float sum_x16(float v) { auto r = __builtin_amdgcn_permlane16_swap(__float_as_uint(v), __float_as_uint(v), false, false); return __uint_as_float(r[0]) + __uint_as_float(r[1]); }
__device__ __forceinline__ float sum_x32(float v) { auto r = __builtin_amdgcn_permlane32_swap(__float_as_uint(v), __float_as_uint(v), false, false); return __uint_as_float(r[0]) + __uint_as_float(r[1]); }
__device__ __forceinline__ float max_x16(float v) { auto r = __builtin_amdgcn_permlane16_swap(__float_as_uint(v), __float_as_uint(v), false, false); return fmaxf(__uint_as_float(r[0]), __uint_as_float(r[1])); }
__device__ __forceinline__ float max_x32(float v) { auto r = __builtin_amdgcn_permlane32_swap(__float_as_uint(v), __float_as_uint(v), false, false); return fmaxf(__uint_as_float(r[0]), __uint_as_float(r[1])); }
__device__ __forceinline__ float sum_rows(float v) { v += dpp<ROR8>(v); return sum_x32(sum_x16(v)); }
__device__ __forceinline__ float max_rows(float v) { v = fmaxf(v, dpp<ROR8>(v)); return max_x32(max_x16(v)); }
__device__ __forceinline__ float dot2(u32 a, u32 b, float c) { return __builtin_amdgcn_fdot2_f32_bf16(__builtin_bit_cast(bf16x2, a), __builtin_bit_cast(bf16x2, b), c, false); }
__device__ __forceinline__ f32x4 mfma(u32x4 a, u32x4 b, f32x4 c) {
  return __builtin_amdgcn_mfma_f32_16x16x32_bf16(__builtin_bit_cast(bf16x8, a), __builtin_bit_cast(bf16x8, b), c, 0, 0, 0);
}
__device__ __forceinline__ u32x4 onehot(u32 zz, u32x4 mask) { u32x4 z = {zz, zz, zz, zz}; return z & mask; }
__device__ __forceinline__ void wg_barrier() { asm volatile("s_waitcnt lgkmcnt(0)\n\ts_barrier" ::: "memory"); }
__device__ __forceinline__ void fence_c() { asm volatile("" ::: "memory"); }
template <class T> __device__ __forceinline__ void arrive(const T& v) { asm volatile("" ::"v"(v)); }
#define SB() __builtin_amdgcn_sched_barrier(0)
__device__ __forceinline__ void pace(u32 due) {
  asm volatile(".Lw_%=:\n\ts_memrealtime vcc\n\ts_waitcnt lgkmcnt(0)\n\ts_lshl_b32 vcc_lo, vcc_lo, 4\n\ts_sub_u32 vcc_lo, vcc_lo, %0\n\ts_cmp_ge_i32 vcc_lo, 0\n\t"
               "s_cbranch_scc1 .Le_%=\n\ts_sleep 1\n\ts_branch .Lw_%=\n.Le_%=:" ::"s"(due) : "scc", "vcc");
}
template <class B, class G> __device__ __forceinline__ u32 gather_spin(B&& bad, G&& gather) {
  u32 n = 0;
  do { fence_c(); gather(); ++n; } while (bad() && n < SPIN_MAX);
  return n - 1;
}
template <class T> __device__ __forceinline__ u32 lds_addr(T* p) { return (u32)(size_t)(__attribute__((address_space(3))) T*)p; }
__device__ __forceinline__ float fold16(float a, float b) { auto r = __builtin_amdgcn_permlane16_swap(__float_as_uint(a), __float_as_uint(b), false, false); return __uint_as_float(r[0]) + __uint_as_float(r[1]); }
__device__ __forceinline__ float fold32(float a, float b) { auto r = __builtin_amdgcn_permlane32_swap(__float_as_uint(a), __float_as_uint(b), false, false); return __uint_as_float(r[0]) + __uint_as_float(r[1]); }
__device__ __forceinline__ float fold4(f32x4 v) { return fold32(fold16(v[0], v[1]), fold16(v[2], v[3])); }
__device__ __forceinline__ float fold4(u32x4 g) { f32x4 v = {val(g[0]), val(g[1]), val(g[2]), val(g[3])}; return fold4(v); }

struct Lds {
  float part1[16 * 192];
  float part2[16 * 192];
  float part3[16 * 64];
  unsigned short qh[256];
  u32x4 fin;
  u32 f1[8];
  u32x2 fm[8];
  u32 fq, ff;
  u32 pad[16384];
};

template <int LD> __device__ __forceinline__ u32 merge_at(u32 lane) { return ((lane >> 4) * 4 * LD + (lane & 15) * 4) * 4; }
template <int LD> __device__ __forceinline__ float merge16(const float* p, u32 at) {
  const float* a = (const float*)((const char*)p + at);
  return fold4((*(const f32x4*)a + *(const f32x4*)(a + LD)) + (*(const f32x4*)(a + 2 * LD) + *(const f32x4*)(a + 3 * LD)));
}

constexpr int WIN = 3, SW = 8, WIN2 = 5, TW = 4;
constexpr int CQ = 22, C2 = 47;
constexpr int XS = 3, TG = 16;
constexpr int TB = 16;
constexpr u32 tau = 500, taub = 440, tau2 = 470;
constexpr u32 lead = 700;
constexpr u32 gd = 8000, md = 18700, od = 2800, pd = 4800;

__global__ void __launch_bounds__(576, 3)
fused_decode(const void* x_, const void* kc_, const void* vc_, const void* wq_, const void* wo_, const void* wg_, const void* wu_, const void* wd_,
             const void* gw_, void* out_, void* ws_) {
  u64x8 ka; u64x2 kb; u64 kw, tw;
  asm volatile("s_load_dwordx16 s[76:91], %0, 0\n\ts_load_dwordx4 s[92:95], %0, 0x40\n\ts_load_dwordx2 s[96:97], %0, 0x50\n\ts_memrealtime s[98:99]"
               ::"s"(__builtin_amdgcn_kernarg_segment_ptr()) : "s76", "s77", "s78", "s79", "s80", "s81", "s82", "s83", "s84", "s85", "s86", "s87", "s88", "s89", "s90", "s91", "s92", "s93", "s94", "s95", "s96", "s97", "s98", "s99");
  SB();
  constexpr u32 UQ = 256, UO = 256, UG = 256, UP = 256, UT = 128, UM = 128;
  constexpr u32 WS_T = 4096, WS_M = WS_T + 256 * UT, WS_Q = WS_M + 256 * UM, WS_G = WS_Q + 288 * UQ, WS_O = WS_G + 96 * UG, WS_P = WS_O + 768 * UO, WS_END = WS_P + 240 * UP;
  __shared__ __attribute__((aligned(16))) Lds L;
  const u32 b = blockIdx.x, grp = b & 7, j = b >> 3;
  const u32 h = grp * 4 + (j & 3), s = j >> 2;
  const bool proj = j < 30;
  const u32 j10 = (j * 26) >> 8, rho = ((grp + XS) & 7) * 3 + j10, ell = j - j10 * 10;
  const u32 t = threadIdx.x, lane = t & 63, w = __builtin_amdgcn_readfirstlane(t >> 6);
  u64 pc; u32 len;
  asm volatile("s_getpc_b64 %0\n.Lpf_pc%=:\n\ts_mov_b32 %1, .Lfused_end-.Lpf_pc%=" : "=s"(pc), "=s"(len));
  const u32 pf = ld32<0>(mkrs((const void*)pc, j >= 30 ? len : 0u), t * 128, 0);
  SB();
  const u32 r = lane >> 3, c = lane & 7, q4 = lane >> 4;
  const u32 ls = ell >= 5, kc5 = ell - ls * 5;
  u32 s1_s[3], s1_d[3], s1_v[3];
  for (int a = 0; a < 3; a++) {
    const u32 n = kc5 * 3 + a, line = (n * 52) >> 8, row = (n - line * 5) * 128 + w * 16;
    s1_d[a] = line < 2 ? 12288 : 3072;
    s1_s[a] = row * s1_d[a] + (line < 2 ? (rho * 4 + ls + line * 2) * 128 : rho * 128);
    s1_v[a] = r * 2 * s1_d[a] + c * 16;
  }
  u32 kv_s = (h * SK + (SK - W) + (s * 8 + w) * 8) * 384u, kv_v = r * 384 + c * 16;
  u32 nq = proj && w < 8 ? 640u * 12288u : 0u, ng = proj ? 640u * 3072u : 0u, xn = kc5 * 3 + (c & 3), x_v = (xn - 5 * ((xn * 52) >> 8)) * 256 + r * 4;
  u32 n10 = kc5 < 4 ? nq : nq >> 2;
  asm volatile("" : "+s"(n10) : "s"(s1_s[0]), "s"(kv_s), "s"(nq), "s"(ng), "v"(s1_v[0]), "v"(s1_v[1]), "v"(kv_v), "v"(x_v));
  SB();
  asm volatile("s_waitcnt lgkmcnt(0)\n\ts_cmp_gt_u32 %4, 4\n\ts_cselect_b64 s[86:87], s[88:89], s[86:87]\n\ts_cmp_lt_u32 %5, 4\n\ts_cselect_b64 s[88:89], s[82:83], s[86:87]"
               : "=&{s[76:91]}"(ka), "=&{s[92:95]}"(kb), "=&{s[96:97]}"(kw), "=&{s[98:99]}"(tw) : "s"(ell), "s"(kc5) : "scc");
  const void *x = (void*)ka[0], *kc = (void*)ka[1], *vc = (void*)ka[2], *wq = (void*)ka[3], *wo = (void*)ka[4], *wgu = (void*)ka[5], *w10 = (void*)ka[6], *wd = (void*)ka[7];
  const void *gw = (void*)kb[0], *out = (void*)kb[1], *ws = (void*)kw;
  const u32 t0 = (u32)tw;
  const rsrc_t rw = mkrs(ws, WS_END), rx = mkrs(x, 1280u), r10 = mkrs(w10, n10);
  const u32x4 first = ld128<NT>(r10, s1_v[0], s1_s[0]);
  SB();
  const u32 x2 = ld32<0>(rx, x_v, w * 32);
  const u32 epoch = ld32<SC1>(rw, WS_E, w == 8 ? 0 : WS_END);
  SB();
  asm volatile("s_cmp_lg_u32 %0, 8\n\ts_cbranch_scc1 .Lcl_%=\n\tds_write_b32 %1, %2\n\ts_waitcnt lgkmcnt(0)\n\ts_barrier\n.Lcl_%=:"
               ::"s"(w), "v"(lds_addr(L.f1) + lane * 4), "v"(0u) : "scc", "memory");
  const u32 o_s = WS_O + (h * 24 + s) * UO, p_s = WS_P + (ell * 24 + rho) * UP;
  const u32 pub_v = lane < 16 ? lane * 16 : OOB;

  if (w == 8) {
  const rsrc_t rgw = mkrs(gw, 1280u);
  const u32x4 gx = ld128<0>(rx, lane * 16, 0), gy = ld128<0>(rgw, lane * 16, 0);
  const u32 gx1 = ld32<0>(rx, lane * 4, 1024), gy1 = ld32<0>(rgw, lane * 4, 1024);
  SB();
  arrive(pf); arrive(first); arrive(x2);
  const u32 tag = __builtin_amdgcn_readfirstlane(epoch) + TSTEP_, tagb = (tag >> q4 * 8) & 0xffu;
  const u32 qa = WS_Q + (rho * 4 + ls) * 3 * UQ, qb = qa + 2 * 3 * UQ, gu = WS_G + (rho * 2 + ls) * 2 * UG;
  const u32 dst0 = !proj ? WS_END : kc5 < 2 ? qa + kc5 * UQ : kc5 < 4 ? qb + (kc5 - 1) * UQ : gu + UG;
  const u32 dst1 = !proj ? WS_END : kc5 == 1 ? qb : kc5 == 3 ? gu : WS_END;
  const u32 m_s = WS_M + h * 8 * UM, half = lane < 8 ? lane * 16 : OOB;
  const u32x4 tag4 = rows4(tagb), tt = {t0, tag, tag, tag};
  st_quad(rw, half, j == TG ? WS_T + (j * 8 + grp) * UT : WS_END, tt);
  float gp = rbf(blo(gx1) * blo(gy1)) + rbf(bhi(gx1) * bhi(gy1));
  for (int k = 0; k < 4; k++) gp += rbf(blo(gx[k]) * blo(gy[k])) + rbf(bhi(gx[k]) * bhi(gy[k]));
  const float gate = rbf(__builtin_amdgcn_rcpf(1.f + __expf(-rbf(sum_rows(sum8(gp))))));
  { u32 f;
    asm volatile(".Lp1_%=:\n\ts_sleep 1\n\tds_read_b32 %0, %1\n\ts_waitcnt lgkmcnt(0)\n\tv_cmp_eq_u32 vcc, 0, %0\n\ts_cbranch_vccnz .Lp1_%="
                 : "=&v"(f) : "v"(lds_addr(&L.f1[c])) : "vcc", "memory"); }
  { const float m0 = merge16<192>(L.part1, merge_at<192>(lane)), m1 = merge16<192>(L.part1, merge_at<192>(lane) + 256), m2 = merge16<192>(L.part1, merge_at<192>(lane) + 512);
    st_quad(rw, pub_v, dst0, rows4(tagged(m0 + (kc5 == 3 ? 0.f : m1) + (kc5 & 1 ? 0.f : m2), tagb)));
    st_quad(rw, pub_v, dst1, rows4(tagged(m2 + (kc5 == 3 ? m1 : 0.f), tagb))); }
  const bool lq = lane < 48, third = !lq || ((h * 3 + q4) & 2) != 0;
  const u32 g1_v = (lq ? WS_Q + (h * 3 + q4) * 3 * UQ : proj ? WS_G + rho * 4 * UG : OOB) + (lane & 15) * 16;
  const u32 gv[4] = {g1_v, g1_v, third ? g1_v : OOB, lq ? OOB : g1_v};
  const u32 want0 = lq || proj ? tag : 0u, want[4] = {want0, want0, third ? want0 : 0u, lq ? 0u : want0};
  u32x4 ga[4];
  u32x2 tb;
  pace((t0 << 4) + gd);
  tb = ld64<SC1>(rw, c * UT, WS_T + TG * 8 * UT);
  gather_spin([&]() { u32 bad = 0; for (int k = 0; k < 4; k++) bad |= tag_of(ga[k]) ^ want[k]; return bad != 0; },
                       [&]() { for (int k = 0; k < 4; k++) ga[k] = ld128<SC1>(rw, gv[k], k * UQ); });
  float y[4];
  for (int k = 0; k < 4; k++) {
    const float u = rbf(mid(ga[0][k]) + mid(ga[1][k]) + (lq ? mid(ga[2][k]) : 0.f));
    const float v = rbf(mid(ga[2][k]) + mid(ga[3][k]));
    const float hv = rbf(u * __builtin_amdgcn_rcpf(1.f + __expf(-1.5957691216057308f * (u + 0.044715f * u * u * u)))) * v;
    y[k] = lq ? u : hv;
  }
  { u32x2 v = {pk_bf16(y[0], y[1]), pk_bf16(y[2], y[3])}; *(u32x2*)&L.qh[lane * 4] = v; }
  int dt = tb[1] == tag ? min(max((int)(tb[0] - t0), -64), 64) : 0;
  dt += dppu<QP1>(dt); dt += dppu<QP2>(dt); dt += dppu<HMIR>(dt);
  const u32 base = ((t0 << 4) + __builtin_amdgcn_readfirstlane(dt) * 2 - 1) | 1u;
  fence_c();
  if (lane == 0) L.fq = base;
  fence_c();
  u32x2 mw;
  asm volatile(".Lpm_%=:\n\ts_sleep 2\n\tds_read_b64 %0, %1\n\ts_waitcnt lgkmcnt(0)\n\tv_cmp_gt_u64 vcc, %2, %0\n\ts_cbranch_vccnz .Lpm_%="
               : "=&v"(mw) : "v"(lds_addr(&L.fm[c])), "s"(1ull << 32) : "vcc", "memory");
  { u32x4 v = {mw[0] & 0xffffff00u, mw[1] & 0xffffff00u, 0u, 0u}; st_quad(rw, half, m_s + s * UM, v | tag4); }
  u32x4 ml;
  { const rsrc_t rc = mkrs((const void*)pc, j >= 30 ? len : 0u); u32 a = 0; for (int k = 0; k < 3; k++) a |= ld32<0>(rc, lane * 128, k * 8192); arrive(a); }
  pace(base + md);
  gather_spin([&]() { return tag_of(ml) != tag; }, [&]() { ml = ld128<SC1>(rw, r * UM + c * 16, m_s); });
  const float mh = max_rows(max8(bhi(ml[0])));
  const float lh = sum_rows(sum8(__expf(bhi(ml[0]) - mh) * mid(ml[1])));
  if (lane == 0) { u32x4 v = {__float_as_uint(mh), __float_as_uint(gate * __builtin_amdgcn_rcpf(lh)), __float_as_uint(rbf(1.f - gate)), tag}; L.fin = v; }
  fence_c();
  if (lane == 0) L.ff = 1;
  fence_c();
  } else {
  const rsrc_t rk = mkrs(kc, 32u * SK * 384u), rv = mkrs(vc, 32u * SK * 384u);
  const rsrc_t r11 = mkrs(kc5 < 3 ? wq : wgu, kc5 < 3 ? nq : ng), ro = mkrs(wo, nq), rd = mkrs(wd, ng);
  u32x4 buf[NS];
  u32 due = (t0 << 4) - lead + w * taub / 8;
  auto stream = [&](auto I) {
    constexpr int i = I;
    if constexpr (i > 0 && i < T0) { due += i < TB ? tau : taub; pace(due); SB(); }
    if constexpr (i >= T0 && i < T0 + TW - 1) { pace(due + (i + 1 - T0) * tau2); SB(); }
    if constexpr (i < K0) {
      constexpr int a = i % 3;
      buf[i] = ld128<NT>(a ? r11 : r10, s1_v[a ? 1 : 0], s1_s[a] + (i / 3) * s1_d[a]);
    } else if constexpr (i < T0) {
      constexpr int k = (i - K0) % 24;
      buf[i] = ld128<NT>(i < V0 ? rk : rv, kv_v + (k % 3) * 128, kv_s + (k / 3) * (512 * 384));
    } else if constexpr (i == T0) {
      buf[i] = ld128<NT>(rd, r * 1280 + c * 16, (rho * 64 + w * 8) * 1280 + ell * 128);
    } else if constexpr (i < NS) {
      buf[i] = ld128<NT>(ro, (q4 + (r & 1) * 16) * 1280 + c * 16, (rho * 256 + w * 32 + 4 * (i - T0 - 1)) * 1280 + ell * 128);
    }
    SB();
  };
  buf[0] = first;
  sfor<1, WIN>(stream);
  wg_barrier();

  u32x4 mask;
  { u32 half = (c & 1) ? 0xffff0000u : 0x0000ffffu;
    for (int k = 0; k < 4; k++) mask[k] = (c >> 1) == k ? half : 0u; }
  const bool dvalid = (q4 >> 1) == (r & 1);
  const u32 dsrc = 2 * w + (q4 >> 1), dcol = c * 8 + (q4 & 1) * 4;

  u32 tag;
  u32x4 qv[3], a7;
  u32 tagb, at2, pw2, o_w;
  float sc[8], m, p7, scale, rest;
  u32 pp[7];
  const f32x4 zero4 = {0, 0, 0, 0};
  f32x4 acc[3] = {zero4, zero4, zero4};
  auto qk = [&](auto G) {
    constexpr int g = G;
    float a0 = 0.f, a1 = 0.f, a2 = 0.f;
    for (int e = 0; e < 4; e++) { a0 = dot2(buf[K0 + 3 * g][e], qv[0][e], a0); a1 = dot2(buf[K0 + 3 * g + 1][e], qv[1][e], a1); a2 = dot2(buf[K0 + 3 * g + 2][e], qv[2][e], a2); }
    sc[g] = rbf(rbf(sum8(a0 + a1 + a2)) * 0.07216878364870322f);
  };
  auto pv = [&](auto G) {
    constexpr int g = G;
    const u32x4 a = onehot(pp[g], mask);
    for (int k = 0; k < 3; k++) acc[k] = mfma(a, buf[V0 + 3 * g + k], acc[k]);
  };

  constexpr int CX = CQ + 8 > V0 ? CQ + 8 : V0;
  sfor<0, T0>([&](auto I) {
    constexpr int i = I;
    arrive(buf[i]);
    if constexpr (i == 0) arrive(pf);
    if constexpr (i == 1) arrive(epoch);
    SB();
    if constexpr (i >= T0 - 3) {
      const f32x4 o = mfma(a7, buf[i], acc[i - (T0 - 3)]);
      if (dvalid) *(f32x4*)((char*)L.part2 + pw2 + (i - (T0 - 3)) * 256) = o;
      SB();
    }
    sfor<issued(i - 1, i - 1 < SW ? WIN : WIN2, TW), issued(i, i < SW ? WIN : WIN2, TW)>(stream);
    if constexpr (i == K0 - 1) {
      f32x4 a3[3];
      const u32 xe = __builtin_amdgcn_perm(x2, x2, 0x01000100u), xo = __builtin_amdgcn_perm(x2, x2, 0x03020302u);
      sfor<0, 3>([&](auto K) { constexpr int k = K; a3[k] = mfma(onehot(dppu<k * 0x55>(xo), mask), buf[3 + k], mfma(onehot(dppu<k * 0x55>(xe), mask), buf[k], zero4)); });
      if (dvalid) for (int k = 0; k < 3; k++) *(f32x4*)&L.part1[dsrc * 192 + k * 64 + dcol] = a3[k];
      fence_c();
      if (lane == 0) L.f1[w] = 1;
      fence_c();
    }
    if constexpr (i == CQ) {
      u32 f;
      asm volatile(".Lpq_%=:\n\tds_read_b32 %0, %4\n\tds_read_b128 %1, %5\n\tds_read_b128 %2, %5 offset:128\n\tds_read_b128 %3, %5 offset:256\n\t"
                   "s_waitcnt lgkmcnt(0)\n\tv_cmp_eq_u32 vcc, 0, %0\n\ts_cbranch_vccnz .Lpq_%="
                   : "=&v"(f), "=&v"(qv[0]), "=&v"(qv[1]), "=&v"(qv[2]) : "v"(lds_addr(&L.fq)), "v"(lds_addr(&L.qh[c * 8])) : "vcc", "memory");
      due += __builtin_amdgcn_readfirstlane(f) - (t0 << 4);
    }
    sfor<0, 8>([&](auto G) { constexpr int g = G, ready = K0 + 3 * g + 2; if constexpr (i == (CQ + g > ready ? CQ + g : ready)) qk(G); });
    if constexpr (i == CX) {
      m = sc[0];
      for (int g = 1; g < 8; g++) m = fmaxf(m, sc[g]);
      m = max_rows(m);
      p7 = __expf(sc[7] - m);
      float lsum = p7;
      for (int g = 0; g < 7; g++) { const float p = __expf(sc[g] - m); lsum += p; pp[g] = pk_bf16(p, p); }
      lsum = sum_rows(lsum);
      if (lane == 0) { u32x2 v = {__float_as_uint(m), __float_as_uint(lsum)}; L.fm[w] = v; }
      fence_c();
    }
    if constexpr (i == C2) {
      u32 f; u32x4 v;
      asm volatile(".Lpz_%=:\n\tds_read_b32 %0, %2\n\tds_read_b128 %1, %3\n\ts_waitcnt lgkmcnt(0)\n\tv_cmp_eq_u32 vcc, 0, %0\n\ts_cbranch_vccnz .Lpz_%="
                   : "=&v"(f), "=&v"(v) : "v"(lds_addr(&L.ff)), "v"(lds_addr(&L.fin)) : "vcc", "memory");
      scale = __expf(m - __uint_as_float(v[0])) * __uint_as_float(v[1]);
      rest = __uint_as_float(v[2]);
      tag = __builtin_amdgcn_readfirstlane(v[3]);
      tagb = (tag >> q4 * 8) & 0xffu;
      a7 = onehot(pk_bf16(p7 * scale, p7 * scale), mask);
      at2 = merge_at<192>(lane) + w * 256;
      pw2 = (dsrc * 192 + dcol) * 4;
      o_w = o_s + w * 8 * UO;
      asm volatile("" : "+v"(scale), "+v"(rest), "+v"(at2), "+v"(pw2), "+v"(tagb), "+v"(a7), "+s"(o_w));
    }
    sfor<0, 7>([&](auto G) { constexpr int g = G, ready = V0 + 3 * g + 2; if constexpr (i == (CX + 1 + g > ready ? CX + 1 + g : ready)) pv(G); });
    if constexpr (i == T0 - 4) { for (int k = 0; k < 3; k++) { acc[k] *= scale; asm volatile("" : "+v"(acc[k])); } }
    SB();
  });
  auto publish = [&](auto LD_, const float* p, u32 at, u32 dst) {
    constexpr int LD = LD_;
    const float* a = (const float*)((const char*)p + at);
    const f32x4 a0 = *(const f32x4*)a, a1 = *(const f32x4*)(a + LD), a2 = *(const f32x4*)(a + 2 * LD), a3 = *(const f32x4*)(a + 3 * LD);
    const f32x4 v = (a0 + a1) + (a2 + a3);
    const u32 y = tagged(fold4(v), tagb);
    st_quad(rw, pub_v, dst, rows4(y));
  };
  wg_barrier();
  if (w < 3) publish(std::integral_constant<int, 192>{}, L.part2, at2, o_w);
  if (proj) {
    u32x4 oa, mask3;
    u32 g3_v = r * UO + c * 16, g3_s = WS_O + (rho * 4 + (w >> 1)) * 8 * UO + (w & 1) * 128;
    auto gather3 = [&]() { oa = ld128<SC1>(rw, g3_v, g3_s); };
    for (int k = 0; k < 4; k++) mask3[k] = (r & 1) * 2 + ((c >> 1) & 1) == k ? (c & 1) ? 0xffff0000u : 0x0000ffffu : 0u;
    const u32 hh = L.qh[192 + w * 8 + q4 * 2 + (c >> 2)];
    u32 p3 = ((2 * w + (q4 & 1)) * 64 + c * 8 + (q4 >> 1) * 4) * 4, at3 = merge_at<64>(lane);
    const u32x4 ah = onehot(hh | (hh << 16), mask3);
    const bool keep3 = (q4 & 1) == (r & 1);
    u32 p_w = p_s;
    asm volatile("" : "+v"(p3), "+v"(at3), "+v"(g3_v), "+s"(p_w) : "v"(ah), "s"(g3_s), "s"(__builtin_amdgcn_ballot_w64(keep3)));
    SB();
    f32x4 aa = mfma(ah, buf[T0], zero4);
    aa *= rest;
    asm volatile("" ::"v"(aa));
    SB();
    pace(due - w * taub / 8 + od);
    gather_spin([&]() { return tag_of(oa) != tag; }, gather3);
    float z = fold4(oa);
    z += dpp<ROR8>(z);
    const u32 zz = pk_bf16(z, z);
    sfor<0, 4>([&](auto K) { constexpr int k = K; aa = mfma(onehot(dppu<k * 0x55>(zz), mask3), buf[T0 + 1 + k], aa); });
    if (keep3) *(f32x4*)((char*)L.part3 + p3) = aa;
    wg_barrier();
    if (w == 7) publish(std::integral_constant<int, 64>{}, L.part3, at3, p_w);
  } else {
    const u32 unit = (grp * 2 + (j - 30)) * 5 + w - 3;
    if (w >= 3) {
      u32x4 pa;
      const u32 want = lane < 48 ? tag : 0u;
      auto gather4 = [&]() { pa = ld128<SC1>(rw, lane < 48 ? (lane >> 1) * UP + (lane & 1) * 16 : OOB, WS_P + (unit >> 3) * 24 * UP + (unit & 7) * 32); };
      const u32 o_v = (lane & 14) == 0 ? ((lane & 1) * 4 + q4) * 2 : OOB, oc = unit * 16;
      const rsrc_t rout = mkrs(out, 1280u);
      asm volatile("" ::"v"(o_v), "s"(oc), "v"(want), "s"(rout));
      pace(due - w * taub / 8 + pd);
      gather_spin([&]() { return tag_of(pa) != want; }, gather4);
      float v = fold4(pa);
      v += dpp<QP2>(v); v += dpp<ROR4>(v); v += dpp<ROR8>(v);
      __builtin_amdgcn_raw_buffer_store_b16((unsigned short)pk_bf16(v, v), rout, o_v, oc, 0);
      if (unit == 0) st_word(rw, lane == 0 ? WS_E : OOB, 0, tag);
    }
  }
  }
  asm volatile(".Lfused_end:");
}

extern "C" void kernel_launch(void* const* d_in, const int* in_sizes, int n_in, void* d_out, int out_size, void* d_ws, size_t ws_size,
                              hipStream_t stream) {
  fused_decode<<<256, 576, 0, stream>>>(d_in[0], d_in[1], d_in[2], d_in[3], d_in[4], d_in[6], d_in[7], d_in[8], d_in[9], d_out, d_ws);
}
```

```cpp
#include <hip/hip_runtime.h>
#include <hip/hip_bf16.h>
#include <cstdint>
#include <utility>

typedef unsigned int u32;
typedef unsigned long long u64;
typedef __attribute__((ext_vector_type(4))) unsigned u32x4;
typedef __attribute__((ext_vector_type(2))) unsigned u32x2;
typedef __attribute__((ext_vector_type(8))) unsigned long long u64x8;
typedef __attribute__((ext_vector_type(2))) unsigned long long u64x2;
typedef __attribute__((ext_vector_type(4))) float f32x4;
typedef __attribute__((ext_vector_type(2))) float f32x2;
typedef __attribute__((ext_vector_type(2))) __bf16 bf16x2;
typedef __attribute__((ext_vector_type(8))) __bf16 bf16x8;
typedef __amdgpu_buffer_rsrc_t rsrc_t;

constexpr int SK = 32768, W = 4096;
constexpr int NT = 2, SC1 = 16;
constexpr u32 OOB = 0x80000000u;
constexpr u32 WS_E = 0;
constexpr u32 TSTEP_ = 0x01030507u;
constexpr u32 SPIN_MAX = 1u << 16;
constexpr int K0 = 6, V0 = 30, T0 = 54, NS = 59;
constexpr int issued(int i, int WIN, int TW) {
  int n = i + 1 + WIN;
  if (n > T0) n = i + 1 + TW > T0 ? i + 1 + TW : T0;
  return n > T0 + TW - 1 ? T0 + TW - 1 : n;
}

template <int B, int E, class F> __device__ __forceinline__ void sfor(F&& f) {
  if constexpr (B < E) { f(std::integral_constant<int, B>{}); sfor<B + 1, E>(f); }
}
__device__ __forceinline__ rsrc_t mkrs(const void* p, u32 bytes) { return __builtin_amdgcn_make_buffer_rsrc((void*)p, 0, bytes, 0x00020000); }
template <int AUX> __device__ __forceinline__ u32x4 ld128(rsrc_t r, u32 v, u32 s) { return __builtin_amdgcn_raw_buffer_load_b128(r, v, s, AUX); }
template <int AUX> __device__ __forceinline__ u32x2 ld64(rsrc_t r, u32 v, u32 s) { return __builtin_amdgcn_raw_buffer_load_b64(r, v, s, AUX); }
template <int AUX> __device__ __forceinline__ u32 ld32(rsrc_t r, u32 v, u32 s) { return __builtin_amdgcn_raw_buffer_load_b32(r, v, s, AUX); }
__device__ __forceinline__ void st_word(rsrc_t r, u32 v, u32 s, u32 x) { __builtin_amdgcn_raw_buffer_store_b32(x, r, v, s, SC1); }
__device__ __forceinline__ void st_quad(rsrc_t r, u32 v, u32 s, u32x4 x) { __builtin_amdgcn_raw_buffer_store_b128(x, r, v, s, SC1); }
__device__ __forceinline__ u32x4 rows4(u32 v) {
  auto r = __builtin_amdgcn_permlane32_swap(v, v, false, false);
  auto a = __builtin_amdgcn_permlane16_swap(r[0], r[0], false, false), b = __builtin_amdgcn_permlane16_swap(r[1], r[1], false, false);
  u32x4 x = {a[0], a[1], b[0], b[1]};
  return x;
}
__device__ __forceinline__ u32 tagged(float a, u32 tagb) { return (__float_as_uint(a) & 0xffffff00u) | tagb; }
__device__ __forceinline__ float val(u32 x) { return __uint_as_float(x & 0xffffff00u); }
__device__ __forceinline__ float mid(u32 x) { return __uint_as_float((x & 0xffffff00u) | 0x80u); }
__device__ __forceinline__ u32 tag_of(u32x4 g) { return __builtin_amdgcn_perm(g[1], g[0], 0x0c0c0400u) | __builtin_amdgcn_perm(g[3], g[2], 0x04000c0cu); }
__device__ __forceinline__ u32 pk_bf16(float a, float b) { f32x2 v = {a, b}; return __builtin_bit_cast(u32, __builtin_convertvector(v, bf16x2)); }
__device__ __forceinline__ float rbf(float a) { return __uint_as_float(pk_bf16(a, a) & 0xffff0000u); }
__device__ __forceinline__ float blo(u32 v) { return __uint_as_float(v << 16); }
__device__ __forceinline__ float bhi(u32 v) { return __uint_as_float(v & 0xffff0000u); }
template <int CTRL> __device__ __forceinline__ float dpp(float v) {
  return __uint_as_float(__builtin_amdgcn_update_dpp(0u, __float_as_uint(v), CTRL, 0xf, 0xf, true));
}
constexpr int QP1 = 0xB1, QP2 = 0x4E, HMIR = 0x141, ROR4 = 0x124, ROR8 = 0x128;
template <int CTRL> __device__ __forceinline__ u32 dppu(u32 v) { return __builtin_amdgcn_update_dpp(0u, v, CTRL, 0xf, 0xf, true); }
__device__ __forceinline__ float sum8(float v) { v += dpp<QP1>(v); v += dpp<QP2>(v); v += dpp<HMIR>(v); return v; }
__device__ __forceinline__ float max8(float v) { v = fmaxf(v, dpp<QP1>(v)); v = fmaxf(v, dpp<QP2>(v)); v = fmaxf(v, dpp<HMIR>(v)); return v; }
__device__ __forceinline__ float sum_x16(float v) { auto r = __builtin_amdgcn_permlane16_swap(__float_as_uint(v), __float_as_uint(v), false, false); return __uint_as_float(r[0]) + __uint_as_float(r[1]); }
__device__ __forceinline__ float sum_x32(float v) { auto r = __builtin_amdgcn_permlane32_swap(__float_as_uint(v), __float_as_uint(v), false, false); return __uint_as_float(r[0]) + __uint_as_float(r[1]); }
__device__ __forceinline__ float max_x16(float v) { auto r = __builtin_amdgcn_permlane16_swap(__float_as_uint(v), __float_as_uint(v), false, false); return fmaxf(__uint_as_float(r[0]), __uint_as_float(r[1])); }
__device__ __forceinline__ float max_x32(float v) { auto r = __builtin_amdgcn_permlane32_swap(__float_as_uint(v), __float_as_uint(v), false, false); return fmaxf(__uint_as_float(r[0]), __uint_as_float(r[1])); }
__device__ __forceinline__ float sum_rows(float v) { v += dpp<ROR8>(v); return sum_x32(sum_x16(v)); }
__device__ __forceinline__ float max_rows(float v) { v = fmaxf(v, dpp<ROR8>(v)); return max_x32(max_x16(v)); }
__device__ __forceinline__ float dot2(u32 a, u32 b, float c) { return __builtin_amdgcn_fdot2_f32_bf16(__builtin_bit_cast(bf16x2, a), __builtin_bit_cast(bf16x2, b), c, false); }
__device__ __forceinline__ f32x4 mfma(u32x4 a, u32x4 b, f32x4 c) {
  return __builtin_amdgcn_mfma_f32_16x16x32_bf16(__builtin_bit_cast(bf16x8, a), __builtin_bit_cast(bf16x8, b), c, 0, 0, 0);
}
__device__ __forceinline__ u32x4 onehot(u32 zz, u32x4 mask) { u32x4 z = {zz, zz, zz, zz}; return z & mask; }
__device__ __forceinline__ void wg_barrier() { asm volatile("s_waitcnt lgkmcnt(0)\n\ts_barrier" ::: "memory"); }
__device__ __forceinline__ void fence_c() { asm volatile("" ::: "memory"); }
template <class T> __device__ __forceinline__ void arrive(const T& v) { asm volatile("" ::"v"(v)); }
#define SB() __builtin_amdgcn_sched_barrier(0)
__device__ __forceinline__ void pace(u32 due) {
  asm volatile(".Lw_%=:\n\ts_memrealtime vcc\n\ts_waitcnt lgkmcnt(0)\n\ts_lshl_b32 vcc_lo, vcc_lo, 4\n\ts_sub_u32 vcc_lo, vcc_lo, %0\n\ts_cmp_ge_i32 vcc_lo, 0\n\t"
               "s_cbranch_scc1 .Le_%=\n\ts_sleep 1\n\ts_branch .Lw_%=\n.Le_%=:" ::"s"(due) : "scc", "vcc");
}
template <class B, class G> __device__ __forceinline__ u32 gather_spin(B&& bad, G&& gather) {
  u32 n = 0;
  do { fence_c(); gather(); ++n; } while (bad() && n < SPIN_MAX);
  return n - 1;
}
template <class T> __device__ __forceinline__ u32 lds_addr(T* p) { return (u32)(size_t)(__attribute__((address_space(3))) T*)p; }
__device__ __forceinline__ float fold16(float a, float b) { auto r = __builtin_amdgcn_permlane16_swap(__float_as_uint(a), __float_as_uint(b), false, false); return __uint_as_float(r[0]) + __uint_as_float(r[1]); }
__device__ __forceinline__ float fold32(float a, float b) { auto r = __builtin_amdgcn_permlane32_swap(__float_as_uint(a), __float_as_uint(b), false, false); return __uint_as_float(r[0]) + __uint_as_float(r[1]); }
__device__ __forceinline__ float fold4(f32x4 v) { return fold32(fold16(v[0], v[1]), fold16(v[2], v[3])); }
__device__ __forceinline__ float fold4(u32x4 g) { f32x4 v = {val(g[0]), val(g[1]), val(g[2]), val(g[3])}; return fold4(v); }

struct Lds {
  float part1[16 * 192];
  float part2[16 * 192];
  float part3[16 * 64];
  unsigned short qh[256];
  u32x4 fin;
  u32 f1[8];
  u32x2 fm[8];
  u32 fq, ff;
  u32 pad[16384];
};

template <int LD> __device__ __forceinline__ u32 merge_at(u32 lane) { return ((lane >> 4) * 4 * LD + (lane & 15) * 4) * 4; }
template <int LD> __device__ __forceinline__ float merge16(const float* p, u32 at) {
  const float* a = (const float*)((const char*)p + at);
  return fold4((*(const f32x4*)a + *(const f32x4*)(a + LD)) + (*(const f32x4*)(a + 2 * LD) + *(const f32x4*)(a + 3 * LD)));
}

constexpr int WIN = 3, SW = 8, WIN2 = 5, TW = 4;
constexpr int CQ = 22, C2 = 47;
constexpr int XS = 3, TG = 16;
constexpr int TB = 16;
constexpr u32 tau = 500, taub = 444, tau2 = 470;
constexpr u32 lead = 800;
constexpr u32 gd = 8000, md = 18700, od = 2900, pd = 4600;

__global__ void __launch_bounds__(576, 3)
fused_decode(const void* x_, const void* kc_, const void* vc_, const void* wq_, const void* wo_, const void* wg_, const void* wu_, const void* wd_,
             const void* gw_, void* out_, void* ws_) {
  u64x8 ka; u64x2 kb; u64 kw, tw;
  asm volatile("s_load_dwordx16 s[76:91], %0, 0\n\ts_load_dwordx4 s[92:95], %0, 0x40\n\ts_load_dwordx2 s[96:97], %0, 0x50\n\ts_memrealtime s[98:99]"
               ::"s"(__builtin_amdgcn_kernarg_segment_ptr()) : "s76", "s77", "s78", "s79", "s80", "s81", "s82", "s83", "s84", "s85", "s86", "s87", "s88", "s89", "s90", "s91", "s92", "s93", "s94", "s95", "s96", "s97", "s98", "s99");
  SB();
  constexpr u32 UQ = 256, UO = 256, UG = 256, UP = 256, UT = 128, UM = 128;
  constexpr u32 WS_T = 4096, WS_M = WS_T + 256 * UT, WS_Q = WS_M + 256 * UM, WS_G = WS_Q + 288 * UQ, WS_O = WS_G + 96 * UG, WS_P = WS_O + 768 * UO, WS_END = WS_P + 240 * UP;
  __shared__ __attribute__((aligned(16))) Lds L;
  const u32 b = blockIdx.x, grp = b & 7, j = b >> 3;
  const u32 h = grp * 4 + (j & 3), s = j >> 2;
  const bool proj = j < 30;
  const u32 j10 = (j * 26) >> 8, rho = ((grp + XS) & 7) * 3 + j10, ell = j - j10 * 10;
  const u32 t = threadIdx.x, lane = t & 63, w = __builtin_amdgcn_readfirstlane(t >> 6);
  u64 pc; u32 len;
  asm volatile("s_getpc_b64 %0\n.Lpf_pc%=:\n\ts_mov_b32 %1, .Lfused_end-.Lpf_pc%=" : "=s"(pc), "=s"(len));
  const u32 pf = ld32<0>(mkrs((const void*)pc, j < 2 ? len : 0u), (t - 448) * 128, 0);
  SB();
  const u32 r = lane >> 3, c = lane & 7, q4 = lane >> 4;
  const u32 ls = ell >= 5, kc5 = ell - ls * 5;
  u32 s1_s[3], s1_d[3], s1_v[3];
  for (int a = 0; a < 3; a++) {
    const u32 n = kc5 * 3 + a, line = (n * 52) >> 8, row = (n - line * 5) * 128 + w * 16;
    s1_d[a] = line < 2 ? 12288 : 3072;
    s1_s[a] = row * s1_d[a] + (line < 2 ? (rho * 4 + ls + line * 2) * 128 : rho * 128);
    s1_v[a] = r * 2 * s1_d[a] + c * 16;
  }
  u32 kv_s = (h * SK + (SK - W) + w * 8) * 384u, kv_v = r * 384 + c * 16;
  u32 nq = proj && w < 8 ? 640u * 12288u : 0u, ng = proj ? 640u * 3072u : 0u, xn = kc5 * 3 + (c & 3), x_v = (xn - 5 * ((xn * 52) >> 8)) * 256 + r * 4;
  u32 n10 = kc5 < 4 ? nq : nq >> 2;
  asm volatile("" : "+s"(n10) : "s"(s1_s[0]), "s"(kv_s), "s"(nq), "s"(ng), "v"(s1_v[0]), "v"(s1_v[1]), "v"(kv_v), "v"(x_v));
  SB();
  asm volatile("s_waitcnt lgkmcnt(0)\n\ts_cmp_gt_u32 %4, 4\n\ts_cselect_b64 s[86:87], s[88:89], s[86:87]\n\ts_cmp_lt_u32 %5, 4\n\ts_cselect_b64 s[88:89], s[82:83], s[86:87]"
               : "=&{s[76:91]}"(ka), "=&{s[92:95]}"(kb), "=&{s[96:97]}"(kw), "=&{s[98:99]}"(tw) : "s"(ell), "s"(kc5) : "scc");
  const void *x = (void*)ka[0], *kc = (void*)ka[1], *vc = (void*)ka[2], *wq = (void*)ka[3], *wo = (void*)ka[4], *wgu = (void*)ka[5], *w10 = (void*)ka[6], *wd = (void*)ka[7];
  const void *gw = (void*)kb[0], *out = (void*)kb[1], *ws = (void*)kw;
  const u32 t0 = (u32)tw;
  const rsrc_t rw = mkrs(ws, WS_END), rx = mkrs(x, 1280u), r10 = mkrs(w10, n10);
  const u32x4 first = ld128<NT>(r10, s1_v[0], s1_s[0]);
  SB();
  const u32 x2 = ld32<0>(rx, x_v, w * 32);
  const u32 epoch = ld32<SC1>(rw, WS_E, w == 8 ? 0 : WS_END);
  SB();
  asm volatile("s_cmp_lg_u32 %0, 8\n\ts_cbranch_scc1 .Lcl_%=\n\tds_write_b32 %1, %2\n\ts_waitcnt lgkmcnt(0)\n\ts_barrier\n.Lcl_%=:"
               ::"s"(w), "v"(lds_addr(L.f1) + lane * 4), "v"(0u) : "scc", "memory");
  const u32 o_s = WS_O + (h * 24 + s) * UO, p_s = WS_P + (ell * 24 + rho) * UP;
  const u32 pub_v = lane < 16 ? lane * 16 : OOB;

  if (w == 8) {
  const rsrc_t rgw = mkrs(gw, 1280u);
  const u32x4 gx = ld128<0>(rx, lane * 16, 0), gy = ld128<0>(rgw, lane * 16, 0);
  const u32 gx1 = ld32<0>(rx, lane * 4, 1024), gy1 = ld32<0>(rgw, lane * 4, 1024);
  SB();
  arrive(pf); arrive(first); arrive(x2);
  const u32 tag = __builtin_amdgcn_readfirstlane(epoch) + TSTEP_, tagb = (tag >> q4 * 8) & 0xffu;
  const u32 qa = WS_Q + (rho * 4 + ls) * 3 * UQ, qb = qa + 2 * 3 * UQ, gu = WS_G + (rho * 2 + ls) * 2 * UG;
  const u32 dst0 = !proj ? WS_END : kc5 < 2 ? qa + kc5 * UQ : kc5 < 4 ? qb + (kc5 - 1) * UQ : gu + UG;
  const u32 dst1 = !proj ? WS_END : kc5 == 1 ? qb : kc5 == 3 ? gu : WS_END;
  const u32 m_s = WS_M + h * 8 * UM, half = lane < 8 ? lane * 16 : OOB;
  const u32x4 tag4 = rows4(tagb), tt = {t0, tag, tag, tag};
  st_quad(rw, half, j == TG ? WS_T + (j * 8 + grp) * UT : WS_END, tt);
  float gp = rbf(blo(gx1) * blo(gy1)) + rbf(bhi(gx1) * bhi(gy1));
  for (int k = 0; k < 4; k++) gp += rbf(blo(gx[k]) * blo(gy[k])) + rbf(bhi(gx[k]) * bhi(gy[k]));
  const float gate = rbf(__builtin_amdgcn_rcpf(1.f + __expf(-rbf(sum_rows(sum8(gp))))));
  { u32 f;
    asm volatile(".Lp1_%=:\n\ts_sleep 1\n\tds_read_b32 %0, %1\n\ts_waitcnt lgkmcnt(0)\n\tv_cmp_eq_u32 vcc, 0, %0\n\ts_cbranch_vccnz .Lp1_%="
                 : "=&v"(f) : "v"(lds_addr(&L.f1[c])) : "vcc", "memory"); }
  { const float m0 = merge16<192>(L.part1, merge_at<192>(lane)), m1 = merge16<192>(L.part1, merge_at<192>(lane) + 256), m2 = merge16<192>(L.part1, merge_at<192>(lane) + 512);
    st_quad(rw, pub_v, dst0, rows4(tagged(m0 + (kc5 == 3 ? 0.f : m1) + (kc5 & 1 ? 0.f : m2), tagb)));
    st_quad(rw, pub_v, dst1, rows4(tagged(m2 + (kc5 == 3 ? m1 : 0.f), tagb))); }
  const bool lq = lane < 48, third = !lq || ((h * 3 + q4) & 2) != 0;
  const u32 g1_v = (lq ? WS_Q + (h * 3 + q4) * 3 * UQ : proj ? WS_G + rho * 4 * UG : OOB) + (lane & 15) * 16;
  const u32 gv[4] = {g1_v, g1_v, third ? g1_v : OOB, lq ? OOB : g1_v};
  const u32 want0 = lq || proj ? tag : 0u, want[4] = {want0, want0, third ? want0 : 0u, lq ? 0u : want0};
  u32x4 ga[4];
  u32x2 tb;
  pace((t0 << 4) + gd);
  tb = ld64<SC1>(rw, c * UT, WS_T + TG * 8 * UT);
  gather_spin([&]() { u32 bad = 0; for (int k = 0; k < 4; k++) bad |= tag_of(ga[k]) ^ want[k]; return bad != 0; },
                       [&]() { for (int k = 0; k < 4; k++) ga[k] = ld128<SC1>(rw, gv[k], k * UQ); });
  float y[4];
  for (int k = 0; k < 4; k++) {
    const float u = rbf(mid(ga[0][k]) + mid(ga[1][k]) + (lq ? mid(ga[2][k]) : 0.f));
    const float v = rbf(mid(ga[2][k]) + mid(ga[3][k]));
    const float hv = rbf(u * __builtin_amdgcn_rcpf(1.f + __expf(-1.5957691216057308f * (u + 0.044715f * u * u * u)))) * v;
    y[k] = lq ? u : hv;
  }
  { u32x2 v = {pk_bf16(y[0], y[1]), pk_bf16(y[2], y[3])}; *(u32x2*)&L.qh[lane * 4] = v; }
  int dt = tb[1] == tag ? min(max((int)(tb[0] - t0), -64), 64) : 0;
  dt += dppu<QP1>(dt); dt += dppu<QP2>(dt); dt += dppu<HMIR>(dt);
  const u32 base = ((t0 << 4) + __builtin_amdgcn_readfirstlane(dt) * 2 - 1) | 1u;
  fence_c();
  if (lane == 0) L.fq = base;
  fence_c();
  u32x2 mw;
  asm volatile(".Lpm_%=:\n\ts_sleep 2\n\tds_read_b64 %0, %1\n\ts_waitcnt lgkmcnt(0)\n\tv_cmp_gt_u64 vcc, %2, %0\n\ts_cbranch_vccnz .Lpm_%="
               : "=&v"(mw) : "v"(lds_addr(&L.fm[c])), "s"(1ull << 32) : "vcc", "memory");
  { u32x4 v = {mw[0] & 0xffffff00u, mw[1] & 0xffffff00u, 0u, 0u}; st_quad(rw, half, m_s + s * UM, v | tag4); }
  u32x4 ml;
  { const rsrc_t rc = mkrs((const void*)pc, j >= 30 ? len : 0u); u32 a = 0; for (int k = 0; k < 3; k++) a |= ld32<0>(rc, lane * 128, k * 8192); arrive(a); }
  pace(base + md);
  gather_spin([&]() { return tag_of(ml) != tag; }, [&]() { ml = ld128<SC1>(rw, r * UM + c * 16, m_s); });
  const float mh = max_rows(max8(bhi(ml[0])));
  const float lh = sum_rows(sum8(__expf(bhi(ml[0]) - mh) * mid(ml[1])));
  if (lane == 0) { u32x4 v = {__float_as_uint(mh), __float_as_uint(gate * __builtin_amdgcn_rcpf(lh)), __float_as_uint(rbf(1.f - gate)), tag}; L.fin = v; }
  fence_c();
  if (lane == 0) L.ff = 1;
  fence_c();
  } else {
  const rsrc_t rk = mkrs(kc, 32u * SK * 384u), rv = mkrs(vc, 32u * SK * 384u);
  const rsrc_t r11 = mkrs(kc5 < 3 ? wq : wgu, kc5 < 3 ? nq : ng), ro = mkrs(wo, nq), rd = mkrs(wd, ng);
  u32x4 buf[NS];
  const u32 off = 100 + w * 27, early = 150;
  u32 due = (t0 << 4) - lead + early;
  auto stream = [&](auto I) {
    constexpr int i = I;
    if constexpr (i > 0 && i < T0) { due += i < TB ? tau : taub; pace(due); SB(); }
    if constexpr (i >= T0 && i < T0 + TW - 1) { pace(due + (i + 1 - T0) * tau2); SB(); }
    if constexpr (i < K0) {
      constexpr int a = i % 3;
      buf[i] = ld128<NT>(a ? r11 : r10, s1_v[a ? 1 : 0], s1_s[a] + (i / 3) * s1_d[a]);
    } else if constexpr (i < T0) {
      constexpr int k = (i - K0) % 24;
      buf[i] = ld128<NT>(i < V0 ? rk : rv, kv_v + (k % 3) * 128, kv_s + (k / 3) * (512 * 384) + ((s + (h | 1) * (k / 3)) & 7) * (64 * 384));
    } else if constexpr (i == T0) {
      buf[i] = ld128<NT>(rd, r * 1280 + c * 16, (rho * 64 + w * 8) * 1280 + ell * 128);
    } else if constexpr (i < NS) {
      buf[i] = ld128<NT>(ro, (q4 + (r & 1) * 16) * 1280 + c * 16, (rho * 256 + w * 32 + 4 * (i - T0 - 1)) * 1280 + ell * 128);
    }
    SB();
  };
  buf[0] = first;
  sfor<1, WIN>(stream);
  wg_barrier();

  u32x4 mask;
  { u32 half = (c & 1) ? 0xffff0000u : 0x0000ffffu;
    for (int k = 0; k < 4; k++) mask[k] = (c >> 1) == k ? half : 0u; }
  const bool dvalid = (q4 >> 1) == (r & 1);
  const u32 dsrc = 2 * w + (q4 >> 1), dcol = c * 8 + (q4 & 1) * 4;

  u32 tag;
  u32x4 qv[3], a7;
  u32 tagb, at2, pw2, o_w;
  float sc[8], m, p7, scale, rest;
  u32 pp[7];
  const f32x4 zero4 = {0, 0, 0, 0};
  f32x4 acc[3] = {zero4, zero4, zero4};
  auto qk = [&](auto G) {
    constexpr int g = G;
    float a0 = 0.f, a1 = 0.f, a2 = 0.f;
    for (int e = 0; e < 4; e++) { a0 = dot2(buf[K0 + 3 * g][e], qv[0][e], a0); a1 = dot2(buf[K0 + 3 * g + 1][e], qv[1][e], a1); a2 = dot2(buf[K0 + 3 * g + 2][e], qv[2][e], a2); }
    sc[g] = rbf(rbf(sum8(a0 + a1 + a2)) * 0.07216878364870322f);
  };
  auto pv = [&](auto G) {
    constexpr int g = G;
    const u32x4 a = onehot(pp[g], mask);
    for (int k = 0; k < 3; k++) acc[k] = mfma(a, buf[V0 + 3 * g + k], acc[k]);
  };

  constexpr int CX = CQ + 8 > V0 ? CQ + 8 : V0;
  sfor<0, T0>([&](auto I) {
    constexpr int i = I;
    arrive(buf[i]);
    if constexpr (i == 0) arrive(pf);
    if constexpr (i == 1) arrive(epoch);
    SB();
    if constexpr (i >= T0 - 3) {
      const f32x4 o = mfma(a7, buf[i], acc[i - (T0 - 3)]);
      if (dvalid) *(f32x4*)((char*)L.part2 + pw2 + (i - (T0 - 3)) * 256) = o;
      SB();
    }
    sfor<issued(i - 1, i - 1 < SW ? WIN : WIN2, TW), issued(i, i < SW ? WIN : WIN2, TW)>(stream);
    if constexpr (i == K0 - 1) {
      f32x4 a3[3];
      const u32 xe = __builtin_amdgcn_perm(x2, x2, 0x01000100u), xo = __builtin_amdgcn_perm(x2, x2, 0x03020302u);
      sfor<0, 3>([&](auto K) { constexpr int k = K; a3[k] = mfma(onehot(dppu<k * 0x55>(xo), mask), buf[3 + k], mfma(onehot(dppu<k * 0x55>(xe), mask), buf[k], zero4)); });
      if (dvalid) for (int k = 0; k < 3; k++) *(f32x4*)&L.part1[dsrc * 192 + k * 64 + dcol] = a3[k];
      fence_c();
      if (lane == 0) L.f1[w] = 1;
      fence_c();
    }
    if constexpr (i == CQ) {
      u32 f;
      asm volatile(".Lpq_%=:\n\tds_read_b32 %0, %4\n\tds_read_b128 %1, %5\n\tds_read_b128 %2, %5 offset:128\n\tds_read_b128 %3, %5 offset:256\n\t"
                   "s_waitcnt lgkmcnt(0)\n\tv_cmp_eq_u32 vcc, 0, %0\n\ts_cbranch_vccnz .Lpq_%="
                   : "=&v"(f), "=&v"(qv[0]), "=&v"(qv[1]), "=&v"(qv[2]) : "v"(lds_addr(&L.fq)), "v"(lds_addr(&L.qh[c * 8])) : "vcc", "memory");
      due += __builtin_amdgcn_readfirstlane(f) - (t0 << 4) + off - early;
    }
    sfor<0, 8>([&](auto G) { constexpr int g = G, ready = K0 + 3 * g + 2; if constexpr (i == (CQ + g > ready ? CQ + g : ready)) qk(G); });
    if constexpr (i == CX) {
      m = sc[0];
      for (int g = 1; g < 8; g++) m = fmaxf(m, sc[g]);
      m = max_rows(m);
      p7 = __expf(sc[7] - m);
      float lsum = p7;
      for (int g = 0; g < 7; g++) { const float p = __expf(sc[g] - m); lsum += p; pp[g] = pk_bf16(p, p); }
      lsum = sum_rows(lsum);
      if (lane == 0) { u32x2 v = {__float_as_uint(m), __float_as_uint(lsum)}; L.fm[w] = v; }
      fence_c();
    }
    if constexpr (i == C2) {
      u32 f; u32x4 v;
      asm volatile(".Lpz_%=:\n\tds_read_b32 %0, %2\n\tds_read_b128 %1, %3\n\ts_waitcnt lgkmcnt(0)\n\tv_cmp_eq_u32 vcc, 0, %0\n\ts_cbranch_vccnz .Lpz_%="
                   : "=&v"(f), "=&v"(v) : "v"(lds_addr(&L.ff)), "v"(lds_addr(&L.fin)) : "vcc", "memory");
      scale = __expf(m - __uint_as_float(v[0])) * __uint_as_float(v[1]);
      rest = __uint_as_float(v[2]);
      tag = __builtin_amdgcn_readfirstlane(v[3]);
      tagb = (tag >> q4 * 8) & 0xffu;
      a7 = onehot(pk_bf16(p7 * scale, p7 * scale), mask);
      at2 = merge_at<192>(lane) + w * 256;
      pw2 = (dsrc * 192 + dcol) * 4;
      o_w = o_s + w * 8 * UO;
      asm volatile("" : "+v"(scale), "+v"(rest), "+v"(at2), "+v"(pw2), "+v"(tagb), "+v"(a7), "+s"(o_w));
    }
    sfor<0, 7>([&](auto G) { constexpr int g = G, ready = V0 + 3 * g + 2; if constexpr (i == (CX + 1 + g > ready ? CX + 1 + g : ready)) pv(G); });
    if constexpr (i == T0 - 4) { for (int k = 0; k < 3; k++) { acc[k] *= scale; asm volatile("" : "+v"(acc[k])); } }
    SB();
  });
  auto publish = [&](auto LD_, const float* p, u32 at, u32 dst) {
    constexpr int LD = LD_;
    const float* a = (const float*)((const char*)p + at);
    const f32x4 a0 = *(const f32x4*)a, a1 = *(const f32x4*)(a + LD), a2 = *(const f32x4*)(a + 2 * LD), a3 = *(const f32x4*)(a + 3 * LD);
    const f32x4 v = (a0 + a1) + (a2 + a3);
    const u32 y = tagged(fold4(v), tagb);
    st_quad(rw, pub_v, dst, rows4(y));
  };
  wg_barrier();
  if (w < 3) publish(std::integral_constant<int, 192>{}, L.part2, at2, o_w);
  else __builtin_amdgcn_s_sleep(4);
  SB();
  sfor<T0 + TW - 1, NS>(stream);
  if (proj) {
    u32x4 oa, mask3;
    u32 g3_v = r * UO + c * 16, g3_s = WS_O + (rho * 4 + (w >> 1)) * 8 * UO + (w & 1) * 128;
    auto gather3 = [&]() { oa = ld128<SC1>(rw, g3_v, g3_s); };
    for (int k = 0; k < 4; k++) mask3[k] = (r & 1) * 2 + ((c >> 1) & 1) == k ? (c & 1) ? 0xffff0000u : 0x0000ffffu : 0u;
    const u32 hh = L.qh[192 + w * 8 + q4 * 2 + (c >> 2)];
    u32 p3 = ((2 * w + (q4 & 1)) * 64 + c * 8 + (q4 >> 1) * 4) * 4, at3 = merge_at<64>(lane);
    const u32x4 ah = onehot(hh | (hh << 16), mask3);
    const bool keep3 = (q4 & 1) == (r & 1);
    u32 p_w = p_s;
    asm volatile("" : "+v"(p3), "+v"(at3), "+v"(g3_v), "+s"(p_w) : "v"(ah), "s"(g3_s), "s"(__builtin_amdgcn_ballot_w64(keep3)));
    SB();
    f32x4 aa = mfma(ah, buf[T0], zero4);
    aa *= rest;
    asm volatile("" ::"v"(aa));
    SB();
    pace(due - off + od);
    gather_spin([&]() { return tag_of(oa) != tag; }, gather3);
    float z = fold4(oa);
    z += dpp<ROR8>(z);
    const u32 zz = pk_bf16(z, z);
    sfor<0, 4>([&](auto K) { constexpr int k = K; aa = mfma(onehot(dppu<k * 0x55>(zz), mask3), buf[T0 + 1 + k], aa); });
    if (keep3) *(f32x4*)((char*)L.part3 + p3) = aa;
    wg_barrier();
    if (w == 7) publish(std::integral_constant<int, 64>{}, L.part3, at3, p_w);
  } else {
    const u32 unit = (grp * 2 + (j - 30)) * 5 + w - 3;
    if (w >= 3) {
      const u32 want = lane < 48 ? tag : 0u;
      const u32 g4_v = lane < 48 ? (lane >> 1) * UP + (lane & 1) * 16 : OOB, g4_s = WS_P + (unit >> 3) * 24 * UP + (unit & 7) * 32;
      const u32 o_v = (lane & 14) == 0 ? ((lane & 1) * 4 + q4) * 2 : OOB, oc = unit * 16;
      const rsrc_t rout = mkrs(out, 1280u);
      asm volatile("" ::"v"(o_v), "s"(oc), "v"(want), "s"(rout), "v"(g4_v), "s"(g4_s));
      pace(due - off + pd);
      u32x4 pa, p0 = ld128<SC1>(rw, g4_v, g4_s), p1;
      SB();
      __builtin_amdgcn_s_sleep(8);
      SB();
      for (u32 n = 0;; n++) {
        fence_c();
        p1 = ld128<SC1>(rw, g4_v, g4_s);
        SB();
        pa = p0;
        if (__builtin_amdgcn_ballot_w64(tag_of(p0) != want) == 0) break;
        fence_c();
        p0 = ld128<SC1>(rw, g4_v, g4_s);
        SB();
        pa = p1;
        if (__builtin_amdgcn_ballot_w64(tag_of(p1) != want) == 0 || n >= SPIN_MAX) break;
      }
      float v = fold4(pa);
      v += dpp<QP2>(v); v += dpp<ROR4>(v); v += dpp<ROR8>(v);
      __builtin_amdgcn_raw_buffer_store_b16((unsigned short)pk_bf16(v, v), rout, o_v, oc, 0);
      if (unit == 0) st_word(rw, lane == 0 ? WS_E : OOB, 0, tag);
      SB();
      arrive(p0); arrive(p1);
    }
  }
  }
  asm volatile(".Lfused_end:");
}

extern "C" void kernel_launch(void* const* d_in, const int* in_sizes, int n_in, void* d_out, int out_size, void* d_ws, size_t ws_size,
                              hipStream_t stream) {
  fused_decode<<<256, 576, 0, stream>>>(d_in[0], d_in[1], d_in[2], d_in[3], d_in[4], d_in[6], d_in[7], d_in[8], d_in[9], d_out, d_ws);
}
```
